```python
import torch

def _softmax(x, axis=-1):
    return torch.softmax(x, dim=axis)

def _gelu(x):
    return torch.nn.functional.gelu(x, approximate='tanh')

def _silu(x):
    return torch.nn.functional.silu(x)

def _sigmoid(x):
    return torch.sigmoid(x)

def _layernorm(x, axis=-1, eps=1e-05):
    m = x.mean(dim=axis, keepdim=True)
    v = x.var(dim=axis, keepdim=True, unbiased=False)
    return (x - m) / torch.sqrt(v + eps)

def _scatter_add(tgt, idx, src, axis=0):
    return tgt.clone().index_add_(axis, idx, src)

def _segment_sum(src, seg, n):
    out = torch.zeros((n,) + src.shape[1:], dtype=src.dtype)
    return out.index_add_(0, seg, src)

def _pad(x, pw):
    flat = tuple((v for pair in reversed(pw) for v in pair))
    return torch.nn.functional.pad(x, flat)
kernel_name = 'recurrent_scan__ssm_conv_s4d__mixer_gmlp__prompt'

def setup_inputs():

    _w = lambda a: (lambda t: t.to(torch.bfloat16) if t.is_floating_point() else t)(torch.as_tensor(a))

    def _A_setup():
        Bsz, L, D, N = (1, 2048, 448, 256)
        return {'u': _w((lambda *s: torch.randn(*s, dtype=torch.bfloat16) * 0.05)(Bsz, L, D)), 'log_A_real': _w((lambda *s: torch.randn(*s, dtype=torch.bfloat16) * 6.4)(D, N) * 0.5), 'A_imag': _w((lambda *s: torch.randn(*s, dtype=torch.bfloat16) * 0.05)(D, N) * 3.14), 'C_real': _w((lambda *s: torch.randn(*s, dtype=torch.bfloat16) * 0.05)(D, N) / N ** 0.5), 'C_imag': _w((lambda *s: torch.randn(*s, dtype=torch.bfloat16) * 0.05)(D, N) / N ** 0.5), 'log_dt': _w((lambda *s: torch.randn(*s, dtype=torch.bfloat16) * 0.05)(D) * 0.5 - 4.0), 'D_skip': _w((lambda *s: torch.randn(*s, dtype=torch.bfloat16) * 0.05)(D))}
    _A_in = _A_setup()

    def _B_setup():
        B, S, D, F = (1, 2048, 448, 1792)
        Fh = F // 2
        return {'x': _w((lambda *s: torch.randn(*s, dtype=torch.bfloat16) * 0.05)(B, S, D)), 'w_in': _w((lambda *s: torch.randn(*s, dtype=torch.bfloat16) * 0.4)(D, 2 * Fh)), 'w_sp': _w(0.01 * (lambda *s: torch.randn(*s, dtype=torch.bfloat16) * 0.4)(S, S)), 'b_sp': _w(1.0 + 0.01 * (lambda *s: torch.randn(*s, dtype=torch.bfloat16) * 0.05)(S)), 'w_out': _w((lambda *s: torch.randn(*s, dtype=torch.bfloat16) * 0.05)(Fh, D))}
    _B_in = _B_setup()
    return {'A_u': _A_in['u'], 'A_log_A_real': _A_in['log_A_real'], 'A_A_imag': _A_in['A_imag'], 'A_C_real': _A_in['C_real'], 'A_C_imag': _A_in['C_imag'], 'A_log_dt': _A_in['log_dt'], 'A_D_skip': _A_in['D_skip'], 'B_x': _B_in['x'], 'B_w_in': _B_in['w_in'], 'B_w_sp': _B_in['w_sp'], 'B_b_sp': _B_in['b_sp'], 'B_w_out': _B_in['w_out']}

def reference(A_u, A_log_A_real, A_A_imag, A_C_real, A_C_imag, A_log_dt, A_D_skip, B_x, B_w_in, B_w_sp, B_b_sp, B_w_out):

    def _A_ref(u, log_A_real, A_imag, C_real, C_imag, log_dt, D_skip):
        L = u.shape[1]
        dt = torch.exp(log_dt)
        A = -torch.exp(log_A_real) + 1j * A_imag
        C = (C_real + 1j * C_imag) * dt[:, None]
        pos = (lambda x, dt: x.to(dt))(torch.arange(L), log_dt.dtype)
        P = torch.exp((A * dt[:, None])[:, :, None] * pos[None, None, :])
        K = 2.0 * torch.einsum('dn,dnl->dl', C, P).real
        L2 = 2 * L
        u_t = (lambda x, *d: x.permute(*d))(u, 0, 2, 1)
        Kf = (lambda x, *a, **k: torch.fft.rfft(x.float(), *a, **k))(K, L2)
        Uf = (lambda x, *a, **k: torch.fft.rfft(x.float(), *a, **k))(u_t, L2)
        y = (lambda x, *a, **k: torch.fft.irfft(x, *a, **k).to(torch.bfloat16))(Uf * Kf[None, :, :], L2)[:, :, :L]
        y = (lambda x, *d: x.permute(*d))(y, 0, 2, 1)
        return y + u * D_skip[None, None, :]

    def _B_ref(x, w_in, w_sp, b_sp, w_out):
        B, S, D = x.shape
        F = w_in.shape[-1]
        Fh = F // 2
        z = _gelu(torch.einsum('bsd,df->bsf', x, w_in))
        z1 = z[..., :Fh].clone()
        z2 = _layernorm(z[..., Fh:], axis=-1)
        g = torch.einsum('ts,bsf->btf', w_sp, z2) + b_sp[None, :, None]
        return torch.einsum('bsf,fd->bsd', z1 * g, w_out)
    x = A_u
    xn = _layernorm(x, axis=-1)
    s = _A_ref(xn, A_log_A_real, A_A_imag, A_C_real, A_C_imag, A_log_dt, A_D_skip)
    g = _silu(_B_ref(xn, B_w_in, B_w_sp, B_b_sp, B_w_out))
    return x + s * g
```

```cpp
#include <hip/hip_runtime.h>
#include <hip/hip_bf16.h>
#include <stdint.h>

typedef unsigned short u16;
typedef unsigned int u32;
typedef unsigned long long u64;
#define LAS __attribute__((address_space(3)))
#define DI __device__ __forceinline__
typedef __attribute__((ext_vector_type(2))) __bf16 bf16x2;
typedef __attribute__((ext_vector_type(8))) __bf16 bf16x8;
typedef __attribute__((ext_vector_type(2))) _Float16 f16x2;
typedef __attribute__((ext_vector_type(8))) _Float16 f16x8;
typedef __attribute__((ext_vector_type(2))) float f32x2;
typedef __attribute__((ext_vector_type(4))) float f32x4;
typedef __attribute__((ext_vector_type(16))) float f32x16;
typedef __attribute__((ext_vector_type(4))) short s16x4;
typedef __attribute__((ext_vector_type(2))) u32 u32x2;
typedef __attribute__((ext_vector_type(4))) u32 u32x4;

constexpr int SL = 2048;
constexpr int DM = 448;
constexpr int FH = 896;
constexpr int NST = 256;

constexpr size_t OFF_XNT = 0;
constexpr size_t OFF_Z1 = OFF_XNT + (size_t)DM * SL * 2;
constexpr size_t OFF_Z2T = OFF_Z1 + (size_t)SL * FH * 2;
constexpr size_t OFF_HT = OFF_Z2T + (size_t)SL * FH * 2;
constexpr size_t OFF_WO = OFF_HT + (size_t)SL * FH * 2;
constexpr size_t OFF_ST = OFF_WO + (size_t)FH * DM * 2;
constexpr size_t OFF_GR = OFF_ST + (size_t)DM * SL * 2;
constexpr size_t OFF_EP = OFF_GR + (size_t)32 * 7 * 64 * 16;

template <int I> struct IC { static constexpr int value = I; };
template <int B, int E, class F> DI void static_for(F&& f) {
    if constexpr (B < E) { f(IC<B>{}); static_for<B + 1, E>(f); }
}
DI float bflo(u32 w) { return __builtin_bit_cast(float, w << 16); }
DI float bfhi(u32 w) { return __builtin_bit_cast(float, w & 0xffff0000u); }
DI float bf1(u16 h) { return __builtin_bit_cast(float, (u32)h << 16); }
DI u32 pkbf(float a, float b) { f32x2 v = {a, b}; bf16x2 r = __builtin_convertvector(v, bf16x2); return __builtin_bit_cast(u32, r); }
DI u32 pkh(float a, float b) { f32x2 v = {a, b}; f16x2 r = __builtin_convertvector(v, f16x2); return __builtin_bit_cast(u32, r); }
DI float rbf(float a) { return bflo(pkbf(a, a)); }
template <int CTRL> DI float dppf(float v) {
    return __builtin_bit_cast(float, __builtin_amdgcn_update_dpp(0, __builtin_bit_cast(int, v), CTRL, 0xF, 0xF, true));
}
DI float red8(float v) { v += dppf<0xB1>(v); v += dppf<0x4E>(v); v += dppf<0x141>(v); return v; }
DI float addx32(float v) {
    u32 b = __builtin_bit_cast(u32, v);
    auto r = __builtin_amdgcn_permlane32_swap(b, b, false, false);
    return __builtin_bit_cast(float, (u32)r[0]) + __builtin_bit_cast(float, (u32)r[1]);
}
DI f32x16 mfma32_bf(u32x4 a, u32x4 b, f32x16 c) {
    return __builtin_amdgcn_mfma_f32_32x32x16_bf16(__builtin_bit_cast(bf16x8, a), __builtin_bit_cast(bf16x8, b), c, 0, 0, 0);
}
DI f32x16 mfma32_h(u32x4 a, u32x4 b, f32x16 c) {
    return __builtin_amdgcn_mfma_f32_32x32x16_f16(__builtin_bit_cast(f16x8, a), __builtin_bit_cast(f16x8, b), c, 0, 0, 0);
}
DI f32x4 mfma16_h(u32x4 a, u32x4 b, f32x4 c) {
    return __builtin_amdgcn_mfma_f32_16x16x32_f16(__builtin_bit_cast(f16x8, a), __builtin_bit_cast(f16x8, b), c, 0, 0, 0);
}
DI u32x2 tr_read(const char* p) {
    s16x4 v = __builtin_amdgcn_ds_read_tr16_b64_v4i16((LAS s16x4*)p);
    return __builtin_bit_cast(u32x2, v);
}
DI u32x4 lds16(const char* p) { return *(const u32x4*)p; }
DI void glds16(const void* sbase, u32 voff, u32 lds_addr) {
    asm volatile("s_mov_b32 m0, %2\n\ts_nop 0\n\tglobal_load_lds_dwordx4 %0, %1" ::"v"(voff), "s"(sbase), "s"(lds_addr) : "memory");
}
template <int N> DI void wait_vm() { asm volatile("s_waitcnt vmcnt(%0)" ::"n"(N) : "memory"); }
DI void gload4x(u32x4& a, u32x4& b, u32x4& c, u32x4& d, u32 voff, const char* base) {
    asm volatile("global_load_dwordx4 %0, %4, %5\n\t"
                 "global_load_dwordx4 %1, %4, %5 offset:1024\n\t"
                 "global_load_dwordx4 %2, %4, %5 offset:2048\n\t"
                 "global_load_dwordx4 %3, %4, %5 offset:3072"
                 : "=&v"(a), "=&v"(b), "=&v"(c), "=&v"(d)
                 : "v"(voff), "s"(base)
                 : "memory");
}
template <int N> DI void wait_vm_tie4(u32x4& a, u32x4& b, u32x4& c, u32x4& d) {
    asm volatile("s_waitcnt vmcnt(%4)" : "+v"(a), "+v"(b), "+v"(c), "+v"(d) : "n"(N) : "memory");
}
DI void wait_lgkm0() { asm volatile("s_waitcnt lgkmcnt(0)" ::: "memory"); }
DI void bar() { asm volatile("" ::: "memory"); __builtin_amdgcn_s_barrier(); asm volatile("" ::: "memory"); }
DI void lbar() { wait_lgkm0(); bar(); }
DI float gelu_tanh(float x) {
    const float c0 = -2.0f * 0.7978845608028654f * 1.4426950408889634f;
    const float c1 = c0 * 0.044715f;
    float t = __builtin_fmaf(x * x, c1, c0);
    float e = __builtin_amdgcn_exp2f(x * t);
    return x * __builtin_amdgcn_rcpf(1.0f + e);
}

constexpr int K1_XSTRIDE = 912;
constexpr int K1_RING = 8;
constexpr int K1_OFF_RING = 64 * K1_XSTRIDE;
constexpr int K1_OFF_PS = K1_OFF_RING + 8 * K1_RING * 1024;
constexpr int K1_OFF_CNT = K1_OFF_PS + 2048;
constexpr int K1_LDS = K1_OFF_CNT + 16;
constexpr u32 TAG_C = 0x9E3779B9u, TAG_X = 0x5BD1E995u;

__global__ __launch_bounds__(512, 2) void k1_kernel(const u16* x, const u16* w_in, const u16* w_out, char* ws) {
    extern __shared__ __attribute__((aligned(16))) char smem[];
    const int tid = threadIdx.x, lane = tid & 63;
    const int wid = __builtin_amdgcn_readfirstlane(tid >> 6);
    const int b = blockIdx.x;
    if (b >= 224) {
        const int u = (b - 224) * 512 + tid;
        if (u < 28 * 2 * 2 * 56) {
            const int o = u % 56, r = u / 56, h = r & 1, s = (r >> 1) & 1, ft = r >> 2;
            u32x4 rows[8];
#pragma unroll
            for (int j = 0; j < 8; ++j) {
                const int f = 32 * ft + 16 * s + 8 * (j >> 2) + 4 * h + (j & 3);
                rows[j] = *(const u32x4*)(w_out + (size_t)f * DM + 8 * o);
            }
            char* wo = ws + OFF_WO;
#pragma unroll
            for (int dd = 0; dd < 8; ++dd) {
                u32x4 v;
#pragma unroll
                for (int k = 0; k < 4; ++k) {
                    const u32 ra = rows[2 * k][dd >> 1], rb = rows[2 * k + 1][dd >> 1];
                    v[k] = (dd & 1) ? ((ra >> 16) | (rb & 0xffff0000u)) : ((ra & 0xffffu) | (rb << 16));
                }
                const int d = 8 * o + dd, dt = d >> 5, dl = d & 31;
                *(u32x4*)(wo + ((size_t)(((dt * 28 + ft) * 2 + s) * 64 + dl + 32 * h)) * 16) = v;
            }
        }
        return;
    }
    const int xcd = b & 7, idx = b >> 3;
    const int p = xcd * 4 + idx / 7, j = idx % 7;
    const u32 lds0 = (u32)(size_t)smem;

    const int row = tid >> 3, t8 = tid & 7;
    u32x4 xv[7];
    u32 epoch;
    {
        const u32 voff = (u32)(((64 * p + row) * DM + t8 * 8) * 2);
        const char* ep = ws + OFF_EP + p * 128;
        const u32 zero = 0;
        asm volatile("global_load_dword %7, %10, %11 sc1\n\t"
                     "global_load_dwordx4 %0, %8, %9\n\t"
                     "global_load_dwordx4 %1, %8, %9 offset:128\n\t"
                     "global_load_dwordx4 %2, %8, %9 offset:256\n\t"
                     "global_load_dwordx4 %3, %8, %9 offset:384\n\t"
                     "global_load_dwordx4 %4, %8, %9 offset:512\n\t"
                     "global_load_dwordx4 %5, %8, %9 offset:640\n\t"
                     "global_load_dwordx4 %6, %8, %9 offset:768"
                     : "=&v"(xv[0]), "=&v"(xv[1]), "=&v"(xv[2]), "=&v"(xv[3]), "=&v"(xv[4]), "=&v"(xv[5]), "=&v"(xv[6]), "=&v"(epoch)
                     : "v"(voff), "s"(x), "v"(zero), "s"(ep)
                     : "memory");
    }
    const int half = wid >> 2, ft = wid & 3;
    const int col0 = (half == 0 ? FH : 0) + 128 * j + 32 * ft;
    const u32 wvoff = (u32)(((lane >> 2) * (2 * FH) + col0 + (lane & 3) * 8) * 2);
    const u32 ring = lds0 + K1_OFF_RING + wid * (K1_RING * 1024);
    static_for<0, K1_RING>([&](auto I) {
        constexpr int ks = I.value;
        glds16((const char*)w_in + (size_t)ks * 16 * (2 * FH) * 2, wvoff, ring + ks * 1024);
    });
    if (tid == 0) *(u32*)(smem + K1_OFF_CNT) = 0;

    asm volatile("s_waitcnt vmcnt(%8)"
                 : "+v"(xv[0]), "+v"(xv[1]), "+v"(xv[2]), "+v"(xv[3]), "+v"(xv[4]), "+v"(xv[5]), "+v"(xv[6]), "+v"(epoch)
                 : "n"(K1_RING) : "memory");
    {
        float xf[56];
#pragma unroll
        for (int i = 0; i < 7; ++i)
#pragma unroll
            for (int k = 0; k < 4; ++k) { xf[8 * i + 2 * k] = bflo(xv[i][k]); xf[8 * i + 2 * k + 1] = bfhi(xv[i][k]); }
        float s = 0.f;
#pragma unroll
        for (int i = 0; i < 56; ++i) s += xf[i];
        s = red8(s);
        const float mean = s * (1.0f / DM);
        float q = 0.f;
#pragma unroll
        for (int i = 0; i < 56; ++i) { const float dlt = xf[i] - mean; q = __builtin_fmaf(dlt, dlt, q); }
        q = red8(q);
        const float mb = rbf(mean), vb = rbf(q * (1.0f / DM));
        const float sd = rbf(__builtin_amdgcn_sqrtf(rbf(vb + 1e-5f)));
        const float rinv = __builtin_amdgcn_rcpf(sd);
#pragma unroll
        for (int i = 0; i < 7; ++i) {
            u32x4 o;
#pragma unroll
            for (int k = 0; k < 4; ++k) {
                const u32 c = pkbf(xf[8 * i + 2 * k] - mb, xf[8 * i + 2 * k + 1] - mb);
                o[k] = pkbf(bflo(c) * rinv, bfhi(c) * rinv);
            }
            *(u32x4*)(smem + row * K1_XSTRIDE + (t8 + 8 * i) * 16) = o;
        }
    }
    lbar();

    if (half == 0) __builtin_amdgcn_s_setprio(2);
    f32x16 acc[2];
#pragma unroll
    for (int i = 0; i < 16; ++i) { acc[0][i] = 0.f; acc[1][i] = 0.f; }
    {
        const int h = lane >> 5, blk = (lane >> 4) & 1, q = (lane & 15) >> 2, pp = lane & 3;
        const char* aptr = smem + K1_OFF_RING + wid * (K1_RING * 1024) + (8 * h + q) * 64 + 32 * blk + 8 * pp;
        const char* bptr = smem + (lane & 31) * K1_XSTRIDE + h * 16;
        u32x4 af[2], bf0[2], bf1_[2];
        auto rd = [&](auto I) {
            constexpr int ks = I.value, cur = ks & 1;
            const u32x2 lo = tr_read(aptr + (ks % K1_RING) * 1024), hi = tr_read(aptr + (ks % K1_RING) * 1024 + 256);
            af[cur] = (u32x4){lo[0], lo[1], hi[0], hi[1]};
            bf0[cur] = lds16(bptr + ks * 32);
            bf1_[cur] = lds16(bptr + 32 * K1_XSTRIDE + ks * 32);
        };
        wait_vm<K1_RING - 1>();
        rd(IC<0>{});
        static_for<0, 28>([&](auto I) {
            constexpr int ks = I.value, cur = ks & 1;
            if constexpr (ks + 1 < 28) {
                constexpr int issued = (ks + K1_RING - 1 < 27) ? ks + K1_RING - 1 : 27;
                wait_vm<issued - (ks + 1)>();
                rd(IC<ks + 1>{});
            }
            __builtin_amdgcn_sched_barrier(0);
            acc[0] = mfma32_bf(af[cur], bf0[cur], acc[0]);
            acc[1] = mfma32_bf(af[cur], bf1_[cur], acc[1]);
            __builtin_amdgcn_sched_barrier(0);
            if constexpr (ks + K1_RING < 28)
                glds16((const char*)w_in + (size_t)(ks + K1_RING) * 16 * (2 * FH) * 2, wvoff, ring + (ks % K1_RING) * 1024);
        });
    }
    __builtin_amdgcn_s_setprio(0);

#pragma unroll
    for (int st = 0; st < 2; ++st)
#pragma unroll
        for (int i = 0; i < 16; i += 2) {
            const u32 c = pkbf(acc[st][i], acc[st][i + 1]);
            const u32 g = pkbf(gelu_tanh(bflo(c)), gelu_tanh(bfhi(c)));
            acc[st][i] = bflo(g); acc[st][i + 1] = bfhi(g);
        }
    const int ftile = 4 * j + ft;
    if (half == 1) {
        char* z1 = ws + OFF_Z1;
#pragma unroll
        for (int st = 0; st < 2; ++st)
#pragma unroll
            for (int hf = 0; hf < 2; ++hf) {
                u32x4 v;
#pragma unroll
                for (int k = 0; k < 4; ++k) {
                    const float lo = acc[st][8 * hf + 2 * k], hi = acc[st][8 * hf + 2 * k + 1];
                    v[k] = (__builtin_bit_cast(u32, lo) >> 16) | __builtin_bit_cast(u32, hi);
                }
                *(u32x4*)(z1 + ((size_t)((ftile * 64 + 2 * p + st) * 2 + hf) * 64 + lane) * 16) = v;
            }
        return;
    }
    {
        float mw[2], m2[2];
#pragma unroll
        for (int st = 0; st < 2; ++st) {
            float s = 0.f;
#pragma unroll
            for (int i = 0; i < 16; ++i) s += acc[st][i];
            s = addx32(s);
            mw[st] = s * (1.0f / 32.0f);
            float q = 0.f;
#pragma unroll
            for (int i = 0; i < 16; ++i) { const float dlt = acc[st][i] - mw[st]; q = __builtin_fmaf(dlt, dlt, q); }
            m2[st] = addx32(q);
        }
        const int h = lane >> 5;
        f32x2 pv = {h ? mw[1] : mw[0], h ? m2[1] : m2[0]};
        *(f32x2*)(smem + K1_OFF_PS + (ft * 64 + lane) * 8) = pv;
    }
    const u32 tagA = epoch + TAG_C, tagB = tagA ^ TAG_X;
    char* gr = ws + OFF_GR + (size_t)p * 7 * 1024;
    const __amdgpu_buffer_rsrc_t grs = __builtin_amdgcn_make_buffer_rsrc(gr, 0, 7 * 1024, 0x00020000);
    {
        wait_lgkm0();
        u32 old = 0;
        if (lane == 0) old = __hip_atomic_fetch_add((LAS u32*)(smem + K1_OFF_CNT), 1u, __ATOMIC_RELAXED, __HIP_MEMORY_SCOPE_WORKGROUP);
        old = __builtin_amdgcn_readfirstlane(old);
        if (old == 3) {
            f32x2 a[4];
#pragma unroll
            for (int w = 0; w < 4; ++w) a[w] = *(const f32x2*)(smem + K1_OFF_PS + (w * 64 + lane) * 8);
            const float mean = (a[0][0] + a[1][0] + a[2][0] + a[3][0]) * 0.25f;
            float m2 = a[0][1] + a[1][1] + a[2][1] + a[3][1];
#pragma unroll
            for (int w = 0; w < 4; ++w) { const float dlt = a[w][0] - mean; m2 = __builtin_fmaf(32.0f * dlt, dlt, m2); }
            u32x4 g = {__builtin_bit_cast(u32, mean), tagA, __builtin_bit_cast(u32, m2), tagB};
            __builtin_amdgcn_raw_buffer_store_b128(g, grs, j * 1024 + lane * 16, 0, 16);
        }
    }
    {
        u16* xnt = (u16*)(ws + OFF_XNT);
        const int g = lane >> 4, q = (lane & 15) >> 2, pp = lane & 3, i16 = lane & 15;
#pragma unroll
        for (int sh = 0; sh < 2; ++sh) {
            const int dbase = 64 * j + 16 * ft;
            const char* a = smem + (32 * sh + 8 * g + q) * K1_XSTRIDE + (dbase + 4 * pp) * 2;
            const u32x2 lo = tr_read(a), hi = tr_read(a + 4 * K1_XSTRIDE);
            *(u32x4*)(xnt + (size_t)(dbase + i16) * SL + 64 * p + 32 * sh + 8 * g) = (u32x4){lo[0], lo[1], hi[0], hi[1]};
        }
    }
    float mb, rinv;
    {
        u32x4 g[7];
        for (int spin = 0; spin < (1 << 22); ++spin) {
            bool ok = true;
#pragma unroll
            for (int w = 0; w < 7; ++w) g[w] = __builtin_amdgcn_raw_buffer_load_b128(grs, w * 1024 + lane * 16, 0, 16);
#pragma unroll
            for (int w = 0; w < 7; ++w) ok = ok && (g[w][1] == tagA) && (g[w][3] == tagB);
            if (__all(ok)) break;
            __builtin_amdgcn_s_sleep(1);
        }
        float mean = 0.f, m2 = 0.f;
#pragma unroll
        for (int w = 0; w < 7; ++w) { mean += __builtin_bit_cast(float, (u32)g[w][0]); m2 += __builtin_bit_cast(float, (u32)g[w][2]); }
        mean *= (1.0f / 7.0f);
#pragma unroll
        for (int w = 0; w < 7; ++w) { const float dlt = __builtin_bit_cast(float, (u32)g[w][0]) - mean; m2 = __builtin_fmaf(128.0f * dlt, dlt, m2); }
        mb = rbf(mean);
        const float vb = rbf(m2 * (1.0f / FH));
        rinv = __builtin_amdgcn_rcpf(rbf(__builtin_amdgcn_sqrtf(rbf(vb + 1e-5f))));
        if (j == 0 && ft == 0 && lane == 0)
            __hip_atomic_store((u32*)(ws + OFF_EP + p * 128), tagA, __ATOMIC_RELAXED, __HIP_MEMORY_SCOPE_AGENT);
    }
    {
        char* tb = smem + K1_OFF_RING + wid * (K1_RING * 1024);
        const int h = lane >> 5;
        u32 mbb = __builtin_bit_cast(u32, mb), rib = __builtin_bit_cast(u32, rinv);
        auto sm = __builtin_amdgcn_permlane32_swap(mbb, mbb, false, false);
        auto sr = __builtin_amdgcn_permlane32_swap(rib, rib, false, false);
#pragma unroll
        for (int st = 0; st < 2; ++st) {
            const float m = __builtin_bit_cast(float, (u32)sm[st]), r = __builtin_bit_cast(float, (u32)sr[st]);
#pragma unroll
            for (int g = 0; g < 4; ++g) {
                u32x2 v;
#pragma unroll
                for (int k = 0; k < 2; ++k) {
                    const u32 c = pkbf(acc[st][4 * g + 2 * k] - m, acc[st][4 * g + 2 * k + 1] - m);
                    v[k] = pkbf(bflo(c) * r, bfhi(c) * r);
                }
                *(u32x2*)(tb + (32 * st + (lane & 31)) * 72 + (8 * g + 4 * h) * 2) = v;
            }
        }
        const int blk = (lane >> 4) & 1, q = (lane & 15) >> 2, pp = lane & 3;
        char* z2t = ws + OFF_Z2T;
#pragma unroll
        for (int ks = 0; ks < 4; ++ks) {
            const char* a = tb + (16 * ks + 8 * h + q) * 72 + 32 * blk + 8 * pp;
            const u32x2 lo = tr_read(a), hi = tr_read(a + 4 * 72);
            *(u32x4*)(z2t + ((size_t)(ftile * 128 + 4 * p + ks) * 64 + lane) * 16) = (u32x4){lo[0], lo[1], hi[0], hi[1]};
        }
    }
}

constexpr int CV_CS = 290 * 16;
constexpr int CV_OFF_KR = 8 * CV_CS;
constexpr int CV_OFF_Y = CV_OFF_KR + 288 * 16;
constexpr int K2_LDS = CV_OFF_Y + 2048 * 4;
constexpr int CV_STG = 32 * 272;

struct cplx { float re, im; };
DI cplx cmul(cplx a, cplx b) { return {__builtin_fmaf(a.re, b.re, -a.im * b.im), __builtin_fmaf(a.re, b.im, a.im * b.re)}; }

DI void gemm2_role(int b, char* smem, const u16* w_sp, const u16* b_sp, char* ws) {
    const int tid = threadIdx.x, lane = tid & 63;
    const int wid = __builtin_amdgcn_readfirstlane(tid >> 6);
    const int xcd = b & 7, idx = b >> 3;
    const int tp = xcd * 4 + idx / 7, j = idx % 7;
    const int ftile = 4 * j + wid;
    const u32 lds0 = (u32)(size_t)smem;
    u32x4 z1v[2][2];
    u32 bsv[2];
    {
        const char* z1 = ws + OFF_Z1 + (size_t)(ftile * 64 + 2 * tp) * 2048;
        const u32 vo = lane * 16, bo = (u32)((64 * tp + (lane & 31)) * 2);
        asm volatile("global_load_dwordx4 %0, %6, %7\n\t"
                     "global_load_dwordx4 %1, %6, %7 offset:1024\n\t"
                     "global_load_dwordx4 %2, %6, %7 offset:2048\n\t"
                     "global_load_dwordx4 %3, %6, %7 offset:3072\n\t"
                     "global_load_ushort %4, %8, %9\n\t"
                     "global_load_ushort %5, %8, %9 offset:64"
                     : "=&v"(z1v[0][0]), "=&v"(z1v[0][1]), "=&v"(z1v[1][0]), "=&v"(z1v[1][1]), "=&v"(bsv[0]), "=&v"(bsv[1])
                     : "v"(vo), "s"(z1), "v"(bo), "s"(b_sp)
                     : "memory");
    }
    u32 wvoff[2];
#pragma unroll
    for (int i = 0; i < 2; ++i) {
        const int r = 16 * wid + 8 * i + (lane >> 3), cc = (lane & 7) ^ ((r >> 1) & 7);
        wvoff[i] = (u32)(((64 * tp + r) * SL + cc * 8) * 2);
    }
    const char* z2base = ws + OFF_Z2T + (size_t)ftile * 128 * 1024;
    const u32 zvoff = lane * 16;
    u32x4 zf[4][4];
    auto issue = [&](auto C) {
        constexpr int c = C.value, slot = c & 3;
        glds16((const char*)w_sp + c * 128, wvoff[0], lds0 + slot * 8192 + wid * 2048);
        glds16((const char*)w_sp + c * 128, wvoff[1], lds0 + slot * 8192 + wid * 2048 + 1024);
        gload4x(zf[slot][0], zf[slot][1], zf[slot][2], zf[slot][3], zvoff, z2base + c * 4096);
    };
    issue(IC<0>{}); issue(IC<1>{}); issue(IC<2>{});
    f32x16 acc[2];
#pragma unroll
    for (int i = 0; i < 16; ++i) { acc[0][i] = 0.f; acc[1][i] = 0.f; }
    const int h = lane >> 5, tl = lane & 31;
    u32 roff[2][4];
#pragma unroll
    for (int tt = 0; tt < 2; ++tt)
#pragma unroll
        for (int kk = 0; kk < 4; ++kk) { const int r = 32 * tt + tl; roff[tt][kk] = r * 128 + (((2 * kk + h) ^ ((r >> 1) & 7)) * 16); }
    static_for<0, 32>([&](auto C) {
        constexpr int c = C.value, slot = c & 3;
        constexpr int younger = (c + 2 <= 31 ? 12 : (c + 1 <= 31 ? 6 : 0));
        wait_vm_tie4<younger>(zf[slot][0], zf[slot][1], zf[slot][2], zf[slot][3]);
        lbar();
        u32x4 wf[4][2];
#pragma unroll
        for (int kk = 0; kk < 4; ++kk) {
            wf[kk][0] = lds16(smem + slot * 8192 + roff[0][kk]);
            wf[kk][1] = lds16(smem + slot * 8192 + roff[1][kk]);
        }
        if constexpr (c + 3 < 32) issue(IC<c + 3>{});
        __builtin_amdgcn_sched_barrier(0);
#pragma unroll
        for (int kk = 0; kk < 4; ++kk) {
            acc[0] = mfma32_bf(zf[slot][kk], wf[kk][0], acc[0]);
            acc[1] = mfma32_bf(zf[slot][kk], wf[kk][1], acc[1]);
        }
        __builtin_amdgcn_sched_barrier(0);
    });
    asm volatile("" : "+v"(z1v[0][0]), "+v"(z1v[0][1]), "+v"(z1v[1][0]), "+v"(z1v[1][1]), "+v"(bsv[0]), "+v"(bsv[1]));
    char* ht = ws + OFF_HT;
#pragma unroll
    for (int tt = 0; tt < 2; ++tt) {
        const float bs = bf1((u16)bsv[tt]);
#pragma unroll
        for (int s = 0; s < 2; ++s) {
            u32x4 v;
#pragma unroll
            for (int k = 0; k < 4; ++k) {
                const u32 g = pkbf(acc[tt][8 * s + 2 * k], acc[tt][8 * s + 2 * k + 1]);
                const u32 g2 = pkbf(bflo(g) + bs, bfhi(g) + bs);
                const u32 z = z1v[tt][s][k];
                v[k] = pkbf(bflo(z) * bflo(g2), bfhi(z) * bfhi(g2));
            }
            *(u32x4*)(ht + ((size_t)(((2 * tp + tt) * 28 + ftile) * 2 + s) * 64 + lane) * 16) = v;
        }
    }
}

DI void conv_role(int d, char* smem, const u16* logA, const u16* Aim, const u16* Cre, const u16* Cim, const u16* logdt,
                  const u16* Dsk, char* ws) {
    const int tid = threadIdx.x, lane = tid & 63;
    const int wid = __builtin_amdgcn_readfirstlane(tid >> 6);
    const u16* xnt = (const u16*)(ws + OFF_XNT) + (size_t)d * SL;
    const u32x4 ucur = *(const u32x4*)(xnt + 8 * tid);
    u32x4 uprev = {0u, 0u, 0u, 0u};
    if (tid > 0) uprev = *(const u32x4*)(xnt + 8 * tid - 8);
    const float lar = bf1(logA[d * NST + tid]), aim = bf1(Aim[d * NST + tid]), cr = bf1(Cre[d * NST + tid]), ci = bf1(Cim[d * NST + tid]);
    const float dt = rbf(expf(bf1(logdt[d])));
    const float dsk = bf1(Dsk[d]);
    *(f32x4*)(smem + CV_OFF_Y + tid * 32) = (f32x4){0.f, 0.f, 0.f, 0.f};
    *(f32x4*)(smem + CV_OFF_Y + tid * 32 + 16) = (f32x4){0.f, 0.f, 0.f, 0.f};
    if (tid < 32) *(u32x4*)(smem + CV_OFF_KR + tid * 16) = (u32x4){0u, 0u, 0u, 0u};

    char* stg = smem + wid * CV_STG;
    {
        const float are = -rbf(expf(lar)) * dt, aimd = aim * dt;
        const float cre = 2.0f * cr * dt, cim = 2.0f * ci * dt;
        float bw = __builtin_fabsf(cre) + __builtin_fabsf(cim);
#pragma unroll
        for (int o = 32; o >= 1; o >>= 1) bw += __shfl_xor(bw, o);
        int ex = (int)((__builtin_bit_cast(u32, bw) >> 23) & 0xff);
        ex = ex < 30 ? 30 : (ex > 230 ? 230 : ex);
        const float scl = __builtin_bit_cast(float, (u32)(127 + 14 + 126 - ex) << 23);
        const float iscl = __builtin_bit_cast(float, (u32)(127 - 14 - 126 + ex) << 23);
        const float a2 = are * 1.4426950408889634f;
        const float thi = aimd * 0.15915494309189535f;
        const float tlo = __builtin_fmaf(aimd, 0.15915494309189535f, -thi) + aimd * 6.4206383e-09f;
        auto cexp = [&](float pw) {
            const float e = __builtin_amdgcn_exp2f(a2 * pw);
            const float th = __builtin_amdgcn_fractf(thi * pw) + tlo * pw;
            return cplx{e * __builtin_amdgcn_cosf(th), e * __builtin_amdgcn_sinf(th)};
        };
        const cplx E1 = cexp(1.0f), E8 = cexp(8.0f), E256 = cexp(256.0f);
        cplx y = {1.0f, 0.0f};
#pragma unroll
        for (int jj = 0; jj < 32; ++jj) {
            *(u32*)(stg + jj * 272 + lane * 4) = pkh(y.re, y.im);
            if (jj < 31) y = cmul(y, E8);
        }
        const int r = lane & 31, h = lane >> 5;
        u32x4 bfr[8];
#pragma unroll
        for (int kk = 0; kk < 8; ++kk) bfr[kk] = lds16(stg + r * 272 + (8 * kk + 4 * h) * 4);
        auto put = [&](int i, cplx v) { *(u32*)(stg + i * 272 + lane * 4) = pkh(v.re, -v.im); };
        const cplx C = {cre * scl, cim * scl};
        cplx xx = C;
#pragma unroll
        for (int i = 0; i < 8; ++i) { put(i, xx); if (i < 7) xx = cmul(xx, E1); }
        const cplx E2 = cmul(E1, E1), E4 = cmul(E2, E2), E512 = cmul(E256, E256);
        xx = cmul(C, E256);
#pragma unroll
        for (int i = 0; i < 4; ++i) { put(8 + i, xx); if (i < 3) xx = cmul(xx, E2); }
        const cplx x512 = cmul(C, E512), x768 = cmul(x512, E256);
        put(12, x512); put(13, cmul(x512, E4)); put(14, x768); put(15, cmul(x768, E4));
        xx = cmul(x512, E512);
#pragma unroll
        for (int i = 0; i < 5; ++i) { put(16 + i, xx); if (i < 4) xx = cmul(xx, E256); }
        f32x16 acc;
#pragma unroll
        for (int i = 0; i < 16; ++i) acc[i] = 0.f;
#pragma unroll
        for (int kk = 0; kk < 8; ++kk) acc = mfma32_h(lds16(stg + r * 272 + (8 * kk + 4 * h) * 4), bfr[kk], acc);
#pragma unroll
        for (int i = 0; i < 12; ++i) {
            const int rowi = (i & 3) + 8 * (i >> 2) + 4 * h;
            *(float*)(stg + (rowi * 32 + r) * 4) = acc[i] * iscl;
        }
        if (lane == 0) *(float*)(stg + 3072) = bw;
    }
    lbar();
    float kinv;
    {
        const float bsum = *(const float*)(smem + 3072) + *(const float*)(smem + CV_STG + 3072) + *(const float*)(smem + 2 * CV_STG + 3072) +
                           *(const float*)(smem + 3 * CV_STG + 3072);
        int ex = (int)((__builtin_bit_cast(u32, bsum) >> 23) & 0xff);
        ex = ex < 30 ? 30 : (ex > 230 ? 230 : ex);
        const float scl = __builtin_bit_cast(float, (u32)(127 + 14 + 126 - ex) << 23);
        kinv = __builtin_bit_cast(float, (u32)(127 - 14 - 126 + ex) << 23);
        float kv[8];
#pragma unroll
        for (int jj = 0; jj < 8; ++jj) {
            const int l = 8 * tid + 7 - jj;
            const int pos = (int)rbf((float)l);
            int i, c;
            if (pos < 256) { i = pos & 7; c = pos >> 3; }
            else if (pos < 512) { const int r = pos - 256; i = 8 + ((r & 7) >> 1); c = r >> 3; }
            else if (pos < 1024) { const int r = pos - 512; i = 12 + 2 * (r >> 8) + ((r & 7) >> 2); c = (r & 255) >> 3; }
            else { const int r = pos - 1024; i = 16 + (r >> 8); c = (r & 255) >> 3; }
            const char* pp = smem + (i * 32 + c) * 4;
            kv[jj] = (*(const float*)pp + *(const float*)(pp + CV_STG) + *(const float*)(pp + 2 * CV_STG) + *(const float*)(pp + 3 * CV_STG)) * scl;
        }
        *(u32x4*)(smem + CV_OFF_KR + (tid + 32) * 16) = (u32x4){pkh(kv[0], kv[1]), pkh(kv[2], kv[3]), pkh(kv[4], kv[5]), pkh(kv[6], kv[7])};
    }
    lbar();
    {
        u32 v[8];
#pragma unroll
        for (int k = 0; k < 4; ++k) { v[k] = pkh(bflo(uprev[k]), bfhi(uprev[k])); v[4 + k] = pkh(bflo(ucur[k]), bfhi(ucur[k])); }
#pragma unroll
        for (int c = 0; c < 8; ++c) {
            u32x4 o;
#pragma unroll
            for (int k = 0; k < 4; ++k) {
                const int e = c + 1 + 2 * k;
                o[k] = (e & 1) ? __builtin_amdgcn_alignbyte(v[(e + 1) >> 1 < 8 ? (e + 1) >> 1 : 7], v[e >> 1], 2) : v[e >> 1];
            }
            *(u32x4*)(smem + c * CV_CS + (tid + 32) * 16) = o;
        }
        *(u32x4*)(smem + (tid >> 5) * CV_CS + (tid & 31) * 16) = (u32x4){0u, 0u, 0u, 0u};
    }
    lbar();
    {
        const int q = lane & 15, kg = lane >> 4, c = lane & 7, pl = (lane >> 3) & 1;
        auto seg = [&](int tt, int e0, int e1) {
            const int m0 = -(16 * tt + 8) + e0, m1 = -(16 * tt + 8) + e1;
            const char* ap = smem + CV_OFF_KR + (q - kg + 32) * 16 - 64 * m0;
            const char* bp = smem + c * CV_CS + (32 * (2 * tt + pl) + kg + 32) * 16 + 64 * m0;
            u32x4 F[4];
#pragma unroll
            for (int i = 0; i < 4; ++i) F[i] = lds16(ap + 256 - 64 * i);
            f32x4 a0 = {0.f, 0.f, 0.f, 0.f}, a1 = {0.f, 0.f, 0.f, 0.f};
            for (int m = m0; m < m1; m += 4) {
#pragma unroll
                for (int i = 0; i < 4; ++i) {
                    const u32x4 fn = lds16(ap - 64 * i);
                    const u32x4 bb = lds16(bp + 64 * i);
                    a0 = mfma16_h(fn, bb, a0);
                    a1 = mfma16_h(F[i], bb, a1);
                    F[i] = fn;
                }
                ap -= 256; bp += 256;
            }
            float* Y = (float*)(smem + CV_OFF_Y) + 256 * (2 * tt + pl) + c + 32 * kg;
#pragma unroll
            for (int r = 0; r < 4; ++r) { atomicAdd(Y + 8 * r, a0[r]); atomicAdd(Y + 128 + 8 * r, a1[r]); }
        };
        if (wid == 0) seg(3, 0, 40);
        else if (wid == 1) { seg(3, 40, 64); seg(0, 0, 16); }
        else if (wid == 2) seg(2, 0, 40);
        else { seg(2, 40, 48); seg(1, 0, 32); }
    }
    lbar();
    {
        const f32x4 y0 = *(const f32x4*)(smem + CV_OFF_Y + tid * 32), y1 = *(const f32x4*)(smem + CV_OFF_Y + tid * 32 + 16);
        u32x4 o;
#pragma unroll
        for (int k = 0; k < 4; ++k) {
            const float ya = k < 2 ? y0[2 * k] : y1[2 * k - 4], yb = k < 2 ? y0[2 * k + 1] : y1[2 * k - 3];
            const u32 y = pkbf(ya * kinv, yb * kinv);
            const u32 sk = pkbf(bflo(ucur[k]) * dsk, bfhi(ucur[k]) * dsk);
            o[k] = pkbf(bflo(y) + bflo(sk), bfhi(y) + bfhi(sk));
        }
        *(u32x4*)((u16*)(ws + OFF_ST) + (size_t)d * SL + 8 * tid) = o;
    }
}

__global__ __launch_bounds__(256, 3) void k2_kernel(const u16* w_sp, const u16* b_sp, const u16* logA, const u16* Aim, const u16* Cre,
                                                    const u16* Cim, const u16* logdt, const u16* Dsk, char* ws) {
    extern __shared__ __attribute__((aligned(16))) char smem[];
    const int b = blockIdx.x;
    if (b < 224) gemm2_role(b, smem, w_sp, b_sp, ws);
    else conv_role(b - 224, smem, logA, Aim, Cre, Cim, logdt, Dsk, ws);
}

constexpr int K3_ROW = 272;
constexpr int K3_LDS = 4 * 64 * K3_ROW;

__global__ __launch_bounds__(256, 1) void k3_kernel(const u16* x, char* ws, u16* out) {
    extern __shared__ __attribute__((aligned(16))) char smem[];
    const int tid = threadIdx.x, lane = tid & 63;
    const int wid = __builtin_amdgcn_readfirstlane(tid >> 6);
    const int b = blockIdx.x, xcd = b & 7, idx = b >> 3;
    const int tp = xcd * 4 + idx / 7, dg = idx % 7;
    const char* wo = ws + OFF_WO + lane * 16;
    const char* ht = ws + OFF_HT + lane * 16;
    u32x4 af[14][2], bfv[14][2];
#pragma unroll
    for (int k = 0; k < 14; ++k) {
        const int ft = 7 * wid + (k >> 1), s = k & 1;
#pragma unroll
        for (int a = 0; a < 2; ++a) {
            af[k][a] = *(const u32x4*)(wo + (size_t)(((2 * dg + a) * 28 + ft) * 2 + s) * 1024);
            bfv[k][a] = *(const u32x4*)(ht + (size_t)(((2 * tp + a) * 28 + ft) * 2 + s) * 1024);
        }
    }
    u32x4 xv[2];
    u16 sv[2][8];
    const u16* st = (const u16*)(ws + OFF_ST);
#pragma unroll
    for (int i = 0; i < 2; ++i) {
        const int u = tid + 256 * i, t = 64 * tp + (u >> 3), d0 = 64 * dg + 8 * (u & 7);
        xv[i] = *(const u32x4*)(x + (size_t)t * DM + d0);
#pragma unroll
        for (int e = 0; e < 8; ++e) sv[i][e] = st[(size_t)(d0 + e) * SL + t];
    }
    __builtin_amdgcn_sched_barrier(0);
    f32x16 acc[2][2];
#pragma unroll
    for (int a = 0; a < 2; ++a)
#pragma unroll
        for (int c = 0; c < 2; ++c)
#pragma unroll
            for (int i = 0; i < 16; ++i) acc[a][c][i] = 0.f;
#pragma unroll
    for (int k = 0; k < 14; ++k)
#pragma unroll
        for (int a = 0; a < 2; ++a)
#pragma unroll
            for (int c = 0; c < 2; ++c) acc[a][c] = mfma32_bf(af[k][a], bfv[k][c], acc[a][c]);
    {
        const int tl = lane & 31, h = lane >> 5;
        char* pw = smem + wid * (64 * K3_ROW);
#pragma unroll
        for (int a = 0; a < 2; ++a)
#pragma unroll
            for (int c = 0; c < 2; ++c)
#pragma unroll
                for (int g = 0; g < 4; ++g)
                    *(f32x4*)(pw + (32 * c + tl) * K3_ROW + (32 * a + 8 * g + 4 * h) * 4) =
                        (f32x4){acc[a][c][4 * g], acc[a][c][4 * g + 1], acc[a][c][4 * g + 2], acc[a][c][4 * g + 3]};
    }
    __syncthreads();
#pragma unroll
    for (int i = 0; i < 2; ++i) {
        const int u = tid + 256 * i, tl = u >> 3, o = u & 7;
        f32x4 s0 = {0.f, 0.f, 0.f, 0.f}, s1 = {0.f, 0.f, 0.f, 0.f};
#pragma unroll
        for (int w = 0; w < 4; ++w) {
            const char* pr = smem + w * (64 * K3_ROW) + tl * K3_ROW + o * 32;
            s0 += *(const f32x4*)pr; s1 += *(const f32x4*)(pr + 16);
        }
        u32x4 ov;
#pragma unroll
        for (int k = 0; k < 4; ++k) {
            const float oa = k < 2 ? s0[2 * k] : s1[2 * k - 4], ob = k < 2 ? s0[2 * k + 1] : s1[2 * k - 3];
            const u32 ob16 = pkbf(oa, ob);
            const float va = bflo(ob16), vb = bfhi(ob16);
            const float ga = va * __builtin_amdgcn_rcpf(1.0f + __builtin_amdgcn_exp2f(-1.4426950408889634f * va));
            const float gb = vb * __builtin_amdgcn_rcpf(1.0f + __builtin_amdgcn_exp2f(-1.4426950408889634f * vb));
            const u32 g = pkbf(ga, gb);
            const u32 pr = pkbf(bf1(sv[i][2 * k]) * bflo(g), bf1(sv[i][2 * k + 1]) * bfhi(g));
            ov[k] = pkbf(bflo(xv[i][k]) + bflo(pr), bfhi(xv[i][k]) + bfhi(pr));
        }
        *(u32x4*)(out + (size_t)(64 * tp + tl) * DM + 64 * dg + 8 * o) = ov;
    }
}

extern "C" void kernel_launch(void* const* d_in, const int* in_sizes, int n_in, void* d_out, int out_size, void* d_ws, size_t ws_size,
                              hipStream_t stream) {
    const u16* x = (const u16*)d_in[0];
    const u16* logA = (const u16*)d_in[1];
    const u16* Aim = (const u16*)d_in[2];
    const u16* Cre = (const u16*)d_in[3];
    const u16* Cim = (const u16*)d_in[4];
    const u16* logdt = (const u16*)d_in[5];
    const u16* Dsk = (const u16*)d_in[6];
    const u16* w_in = (const u16*)d_in[8];
    const u16* w_sp = (const u16*)d_in[9];
    const u16* b_sp = (const u16*)d_in[10];
    const u16* w_out = (const u16*)d_in[11];
    char* ws = (char*)d_ws;
    hipFuncSetAttribute((const void*)k1_kernel, hipFuncAttributeMaxDynamicSharedMemorySize, K1_LDS);
    hipFuncSetAttribute((const void*)k2_kernel, hipFuncAttributeMaxDynamicSharedMemorySize, K2_LDS);
    hipFuncSetAttribute((const void*)k3_kernel, hipFuncAttributeMaxDynamicSharedMemorySize, K3_LDS);
    k1_kernel<<<256, 512, K1_LDS, stream>>>(x, w_in, w_out, ws);
    k2_kernel<<<224 + 448, 256, K2_LDS, stream>>>(w_sp, b_sp, logA, Aim, Cre, Cim, logdt, Dsk, ws);
    k3_kernel<<<224, 256, K3_LDS, stream>>>(x, ws, (u16*)d_out);
}
```

```cpp
#define LAM __attribute__((always_inline))
#include <hip/hip_runtime.h>
#include <hip/hip_bf16.h>
#include <stdint.h>

typedef unsigned short u16;
typedef unsigned int u32;
typedef unsigned long long u64;
#define LAS __attribute__((address_space(3)))
#define DI __device__ __forceinline__
typedef __attribute__((ext_vector_type(2))) __bf16 bf16x2;
typedef __attribute__((ext_vector_type(8))) __bf16 bf16x8;
typedef __attribute__((ext_vector_type(2))) _Float16 f16x2;
typedef __attribute__((ext_vector_type(8))) _Float16 f16x8;
typedef __attribute__((ext_vector_type(2))) float f32x2;
typedef __attribute__((ext_vector_type(4))) float f32x4;
typedef __attribute__((ext_vector_type(16))) float f32x16;
typedef __attribute__((ext_vector_type(4))) short s16x4;
typedef __attribute__((ext_vector_type(2))) u32 u32x2;
typedef __attribute__((ext_vector_type(4))) u32 u32x4;

constexpr int SL = 2048;
constexpr int DM = 448;
constexpr int FH = 896;
constexpr int NST = 256;

constexpr size_t OFF_XNT = 0;
constexpr size_t OFF_Z1 = OFF_XNT + (size_t)DM * SL * 2;
constexpr size_t OFF_Z2T = OFF_Z1 + (size_t)SL * FH * 2;
constexpr size_t OFF_HT = OFF_Z2T + (size_t)SL * FH * 2;
constexpr size_t OFF_WO = OFF_HT + (size_t)SL * FH * 2;
constexpr size_t OFF_ST = OFF_WO + (size_t)FH * DM * 2;
constexpr size_t OFF_GR = OFF_ST + (size_t)DM * SL * 2;
constexpr size_t OFF_EP = OFF_GR + (size_t)32 * 7 * 64 * 16;
constexpr size_t OFF_KR = OFF_EP + 32 * 128;
constexpr size_t OFF_KI = OFF_KR + (size_t)DM * 4096;

#define STAMP(kern, i) do {} while (0)
#define STAMP_DECL do {} while (0)
template <int I> struct IC { static constexpr int value = I; };
template <int B, int E, class F> DI void static_for(F&& f) {
    if constexpr (B < E) { f(IC<B>{}); static_for<B + 1, E>(f); }
}
DI float bflo(u32 w) { return __builtin_bit_cast(float, w << 16); }
DI float bfhi(u32 w) { return __builtin_bit_cast(float, w & 0xffff0000u); }
DI float bf1(u16 h) { return __builtin_bit_cast(float, (u32)h << 16); }
DI u32 pkbf(float a, float b) { f32x2 v = {a, b}; bf16x2 r = __builtin_convertvector(v, bf16x2); return __builtin_bit_cast(u32, r); }
DI u32 pkh(float a, float b) { f32x2 v = {a, b}; f16x2 r = __builtin_convertvector(v, f16x2); return __builtin_bit_cast(u32, r); }
DI float rbf(float a) { return bflo(pkbf(a, a)); }
template <int CTRL> DI float dppf(float v) {
    return __builtin_bit_cast(float, __builtin_amdgcn_update_dpp(0, __builtin_bit_cast(int, v), CTRL, 0xF, 0xF, true));
}
DI float red8(float v) { v += dppf<0xB1>(v); v += dppf<0x4E>(v); v += dppf<0x141>(v); return v; }
DI float addx32(float v) {
    u32 b = __builtin_bit_cast(u32, v);
    auto r = __builtin_amdgcn_permlane32_swap(b, b, false, false);
    return __builtin_bit_cast(float, (u32)r[0]) + __builtin_bit_cast(float, (u32)r[1]);
}
DI f32x16 mfma32_bf(u32x4 a, u32x4 b, f32x16 c) {
    return __builtin_amdgcn_mfma_f32_32x32x16_bf16(__builtin_bit_cast(bf16x8, a), __builtin_bit_cast(bf16x8, b), c, 0, 0, 0);
}
DI f32x16 mfma32_h(u32x4 a, u32x4 b, f32x16 c) {
    return __builtin_amdgcn_mfma_f32_32x32x16_f16(__builtin_bit_cast(f16x8, a), __builtin_bit_cast(f16x8, b), c, 0, 0, 0);
}
DI f32x4 mfma16_h(u32x4 a, u32x4 b, f32x4 c) {
    return __builtin_amdgcn_mfma_f32_16x16x32_f16(__builtin_bit_cast(f16x8, a), __builtin_bit_cast(f16x8, b), c, 0, 0, 0);
}
DI u32x2 tr_read(const char* p) {
    s16x4 v = __builtin_amdgcn_ds_read_tr16_b64_v4i16((LAS s16x4*)p);
    return __builtin_bit_cast(u32x2, v);
}
DI u32x4 lds16(const char* p) { return *(const u32x4*)p; }
DI void glds16(const void* sbase, u32 voff, u32 lds_addr) {
    asm volatile("s_mov_b32 m0, %2\n\ts_nop 0\n\tglobal_load_lds_dwordx4 %0, %1" ::"v"(voff), "s"(sbase), "s"(lds_addr) : "memory");
}
template <int N> DI void wait_vm() { asm volatile("s_waitcnt vmcnt(%0)" ::"n"(N) : "memory"); }
DI void gload4x(u32x4& a, u32x4& b, u32x4& c, u32x4& d, u32 voff, const char* base) {
    asm volatile("global_load_dwordx4 %0, %4, %5\n\t"
                 "global_load_dwordx4 %1, %4, %5 offset:1024\n\t"
                 "global_load_dwordx4 %2, %4, %5 offset:2048\n\t"
                 "global_load_dwordx4 %3, %4, %5 offset:3072"
                 : "=&v"(a), "=&v"(b), "=&v"(c), "=&v"(d)
                 : "v"(voff), "s"(base)
                 : "memory");
}
DI void gload1(u32x4& a, u32 voff, const char* base) {
    asm volatile("global_load_dwordx4 %0, %1, %2" : "=&v"(a) : "v"(voff), "s"(base) : "memory");
}
DI void gload_u16x2(u32& a, u32& b, u32 voff, const char* base) {
    asm volatile("global_load_ushort %0, %2, %3\n\tglobal_load_ushort %1, %2, %3 offset:64" : "=&v"(a), "=&v"(b) : "v"(voff), "s"(base) : "memory");
}
template <int N> DI void wait_vm_tie4(u32x4& a, u32x4& b, u32x4& c, u32x4& d) {
    asm volatile("s_waitcnt vmcnt(%4)" : "+v"(a), "+v"(b), "+v"(c), "+v"(d) : "n"(N) : "memory");
}
#define GST_MODE 0
DI void gst16(void* p, u32x4 v) {
    *(u32x4*)p = v;
}
DI void wait_lgkm0() { asm volatile("s_waitcnt lgkmcnt(0)" ::: "memory"); }
DI void bar() { asm volatile("" ::: "memory"); __builtin_amdgcn_s_barrier(); asm volatile("" ::: "memory"); }
DI void lbar() { wait_lgkm0(); bar(); }
DI float dot2bf(u32 a, u32 b, float c) {
    return __builtin_amdgcn_fdot2_f32_bf16(__builtin_bit_cast(bf16x2, a), __builtin_bit_cast(bf16x2, b), c, false);
}
DI void sched_fence() { __builtin_amdgcn_sched_barrier(0); }
DI float gelu_tanh(float x) {
    const float c0 = -2.0f * 0.7978845608028654f * 1.4426950408889634f;
    const float c1 = c0 * 0.044715f;
    float t = __builtin_fmaf(x * x, c1, c0);
    float e = __builtin_amdgcn_exp2f(x * t);
    return x * __builtin_amdgcn_rcpf(1.0f + e);
}

DI float smul(float a, float b) { float r; asm("v_mul_f32 %0, %1, %2" : "=v"(r) : "v"(a), "v"(b)); return r; }
DI float sfma(float a, float b, float c) { float r; asm("v_fma_f32 %0, %1, %2, %3" : "=v"(r) : "v"(a), "v"(b), "v"(c)); return r; }
DI float sfnma(float a, float b, float c) { float r; asm("v_fma_f32 %0, -%1, %2, %3" : "=v"(r) : "v"(a), "v"(b), "v"(c)); return r; }
DI void gelu4(float& x0, float& x1, float& x2, float& x3) {
    const float c1 = -2.0f * 0.7978845608028654f * 1.4426950408889634f * 0.044715f;
    float t0, t1, t2, t3;
    asm("v_mul_f32 %4, %0, %0\n\tv_mul_f32 %5, %1, %1\n\tv_mul_f32 %6, %2, %2\n\tv_mul_f32 %7, %3, %3\n\t"
        "v_fmaak_f32 %4, %4, %8, 0xc0135761\n\tv_fmaak_f32 %5, %5, %8, 0xc0135761\n\tv_fmaak_f32 %6, %6, %8, 0xc0135761\n\tv_fmaak_f32 %7, %7, %8, 0xc0135761\n\t"
        "v_mul_f32 %4, %4, %0\n\tv_mul_f32 %5, %5, %1\n\tv_mul_f32 %6, %6, %2\n\tv_mul_f32 %7, %7, %3\n\t"
        "v_exp_f32 %4, %4\n\tv_exp_f32 %5, %5\n\tv_exp_f32 %6, %6\n\tv_exp_f32 %7, %7\n\t"
        "v_add_f32 %4, 1.0, %4\n\tv_add_f32 %5, 1.0, %5\n\tv_add_f32 %6, 1.0, %6\n\tv_add_f32 %7, 1.0, %7\n\t"
        "v_rcp_f32 %4, %4\n\tv_rcp_f32 %5, %5\n\tv_rcp_f32 %6, %6\n\tv_rcp_f32 %7, %7\n\t"
        "v_mul_f32 %0, %0, %4\n\tv_mul_f32 %1, %1, %5\n\tv_mul_f32 %2, %2, %6\n\tv_mul_f32 %3, %3, %7"
        : "+v"(x0), "+v"(x1), "+v"(x2), "+v"(x3), "=&v"(t0), "=&v"(t1), "=&v"(t2), "=&v"(t3)
        : "v"(c1));
}
DI f32x2 gelu2(f32x2 x) {
    const float c0 = -2.0f * 0.7978845608028654f * 1.4426950408889634f;
    const float c1 = c0 * 0.044715f;
    const f32x2 t = __builtin_elementwise_fma(x * x, (f32x2){c1, c1}, (f32x2){c0, c0}) * x;
    const f32x2 d = (f32x2){__builtin_amdgcn_exp2f(t[0]), __builtin_amdgcn_exp2f(t[1])} + 1.0f;
    return x * (f32x2){__builtin_amdgcn_rcpf(d[0]), __builtin_amdgcn_rcpf(d[1])};
}

constexpr int CV_STG = 32 * 272;
struct cpx { float re, im; };
template <int OFF0, int OFF1> DI void lds_put2(u32 addr, u32 a, u32 b) {
    asm volatile("ds_write2_b32 %0, %1, %2 offset0:%3 offset1:%4" ::"v"(addr), "v"(a), "v"(b), "n"(OFF0), "n"(OFF1) : "memory");
}
DI void cmul4(cpx (&o)[4], const cpx& a0, const cpx& b0, const cpx& a1, const cpx& b1, const cpx& a2, const cpx& b2, const cpx& a3,
              const cpx& b3) {
    asm("v_mul_f32 %0, %8, %10\n\tv_mul_f32 %2, %12, %14\n\tv_mul_f32 %4, %16, %18\n\tv_mul_f32 %6, %20, %22\n\t"
        "v_mul_f32 %1, %8, %11\n\tv_mul_f32 %3, %12, %15\n\tv_mul_f32 %5, %16, %19\n\tv_mul_f32 %7, %20, %23\n\t"
        "v_fma_f32 %0, -%9, %11, %0\n\tv_fma_f32 %2, -%13, %15, %2\n\tv_fma_f32 %4, -%17, %19, %4\n\tv_fma_f32 %6, -%21, %23, %6\n\t"
        "v_fma_f32 %1, %9, %10, %1\n\tv_fma_f32 %3, %13, %14, %3\n\tv_fma_f32 %5, %17, %18, %5\n\tv_fma_f32 %7, %21, %22, %7"
        : "=&v"(o[0].re), "=&v"(o[0].im), "=&v"(o[1].re), "=&v"(o[1].im), "=&v"(o[2].re), "=&v"(o[2].im), "=&v"(o[3].re), "=&v"(o[3].im)
        : "v"(a0.re), "v"(a0.im), "v"(b0.re), "v"(b0.im), "v"(a1.re), "v"(a1.im), "v"(b1.re), "v"(b1.im), "v"(a2.re), "v"(a2.im),
          "v"(b2.re), "v"(b2.im), "v"(a3.re), "v"(a3.im), "v"(b3.re), "v"(b3.im));
}
DI float exp_acc(float x) {
    const float t = x * 1.44269502f;
    const float r = __builtin_fmaf(x, 1.92596299e-8f, __builtin_fmaf(x, 1.44269502f, -t));
    const float e = __builtin_amdgcn_exp2f(t);
    return __builtin_fmaf(e, r * 0.693147182f, e);
}
DI float wave_sum(float v) {
    v = red8(v);
    v += dppf<0x128>(v);
    const int b = __builtin_bit_cast(int, v);
    return (__builtin_bit_cast(float, __builtin_amdgcn_readlane(b, 0)) + __builtin_bit_cast(float, __builtin_amdgcn_readlane(b, 16))) +
           (__builtin_bit_cast(float, __builtin_amdgcn_readlane(b, 32)) + __builtin_bit_cast(float, __builtin_amdgcn_readlane(b, 48)));
}
DI float kgen_wave(char* stg, int lane, float lar, float aim, float cr, float ci, float ldt, float cabs) {
    const float dt = rbf(exp_acc(ldt));
    const float are = -rbf(exp_acc(lar)) * dt, aimd = aim * dt;
    const float bound = wave_sum(cabs) * (2.0f * dt);
    int ex = (int)((__builtin_bit_cast(u32, bound) >> 23) & 0xff);
    ex = ex < 30 ? 30 : (ex > 230 ? 230 : ex);
    const float scl = __builtin_bit_cast(float, (u32)(127 + 14 + 126 - ex) << 23);
    const float a2 = are * 1.4426950408889634f;
    const float thi = aimd * 0.15915494309189535f;
    const float tlo = __builtin_fmaf(aimd, 0.15915494309189535f, -thi) + aimd * 6.4206383e-09f;
    auto cexp = [&](float pw) LAM {
        const float e = __builtin_amdgcn_exp2f(a2 * pw);
        const float th = __builtin_amdgcn_fractf(thi * pw) + tlo * pw;
        return cpx{e * __builtin_amdgcn_cosf(th), e * __builtin_amdgcn_sinf(th)};
    };
    const cpx E1 = cexp(1.0f), E8 = cexp(8.0f), E256 = cexp(256.0f);
    const u32 sa = (u32)(size_t)stg + lane * 4;
    auto pk = [&](const cpx& v) LAM { return pkh(v.re, v.im); };
    const cpx e1 = {E1.re, -E1.im}, g1 = {E256.re, -E256.im};
    const float cs2 = 2.0f * dt * scl;
    const cpx C = {cr * cs2, -ci * cs2};
    cpx t[4], y[4], u[4];
    cmul4(t, E8, E8, e1, e1, g1, g1, C, g1);
    const cpx E16 = t[0], e2 = t[1], g2 = t[2], c256 = t[3];
    cmul4(t, E16, E8, E16, E16, e2, e1, e2, e2);
    const cpx E32 = t[1], e3 = t[2], e4 = t[3];
    y[0] = cpx{1.0f, 0.0f}; y[1] = E8; y[2] = E16; y[3] = t[0];
    cmul4(t, g2, g1, g2, g2, C, g2, C, e1);
    const cpx g3 = t[0], g4 = t[1], c512 = t[2], r1 = t[3];
#pragma unroll
    for (int jj = 0; jj < 8; ++jj) {
        lds_put2<0, 68>(sa + jj * 1088, pk(y[0]), pk(y[1]));
        lds_put2<136, 204>(sa + jj * 1088, pk(y[2]), pk(y[3]));
        if (jj < 7) { cmul4(u, y[0], E32, y[1], E32, y[2], E32, y[3], E32); y[0] = u[0]; y[1] = u[1]; y[2] = u[2]; y[3] = u[3]; }
    }
    const int r = lane & 31, h = lane >> 5;
    u32x4 bfr[8];
#pragma unroll
    for (int kk = 0; kk < 8; ++kk) bfr[kk] = lds16(stg + r * 272 + (8 * kk + 4 * h) * 4);
    cpx e567[4], g5678[4];
    cmul4(e567, e4, e1, e4, e2, e4, e3, C, g3);
    cmul4(g5678, g4, g1, g4, g2, g4, g3, g4, g4);
    const cpx c768 = e567[3];
    lds_put2<0, 68>(sa, pk(C), pk(r1));
    cmul4(t, C, e2, C, e3, C, e4, C, e567[0]);
    lds_put2<136, 204>(sa, pk(t[0]), pk(t[1]));
    lds_put2<0, 68>(sa + 4 * 272, pk(t[2]), pk(t[3]));
    cmul4(t, C, e567[1], C, e567[2], c256, e2, c256, e4);
    lds_put2<136, 204>(sa + 4 * 272, pk(t[0]), pk(t[1]));
    lds_put2<0, 68>(sa + 8 * 272, pk(c256), pk(t[2]));
    cmul4(u, c256, e567[1], c512, e4, c768, e4, C, g4);
    lds_put2<136, 204>(sa + 8 * 272, pk(t[3]), pk(u[0]));
    lds_put2<0, 68>(sa + 12 * 272, pk(c512), pk(u[1]));
    lds_put2<136, 204>(sa + 12 * 272, pk(c768), pk(u[2]));
    cmul4(t, C, g5678[0], C, g5678[1], C, g5678[2], C, g5678[3]);
    lds_put2<0, 68>(sa + 16 * 272, pk(u[3]), pk(t[0]));
    lds_put2<136, 204>(sa + 16 * 272, pk(t[1]), pk(t[2]));
    *(u32*)(stg + 20 * 272 + lane * 4) = pk(t[3]);
    u32x4 afr[8];
#pragma unroll
    for (int kk = 0; kk < 8; ++kk) afr[kk] = lds16(stg + r * 272 + (8 * kk + 4 * h) * 4);
    sched_fence();
    f32x16 acc;
#pragma unroll
    for (int i = 0; i < 16; ++i) acc[i] = 0.f;
#pragma unroll
    for (int kk = 0; kk < 8; ++kk) acc = mfma32_h(afr[kk], bfr[kk], acc);
#pragma unroll
    for (int g = 0; g < 3; ++g)
        *(f32x4*)(stg + (g * 64 + lane) * 16) = (f32x4){acc[4 * g], acc[4 * g + 1], acc[4 * g + 2], acc[4 * g + 3]};
    return __builtin_bit_cast(float, (u32)(127 - 14 - 126 + ex) << 23);
}
DI void kgen_reduce(const char* pin, char* tout, int tid) {
    if (tid < 192) {
        const char* pp = pin + tid * 16;
        const f32x4 a = *(const f32x4*)pp, b = *(const f32x4*)(pp + CV_STG), c = *(const f32x4*)(pp + 2 * CV_STG), d = *(const f32x4*)(pp + 3 * CV_STG);
        *(f32x4*)(tout + tid * 16) = (a + b) + (c + d);
    }
}
DI float ktab(const char* tin, int idx) {
    const int row = idx >> 5, col = idx & 31;
    return *(const float*)(tin + (((row >> 3) * 64 + col + 32 * ((row >> 2) & 1)) * 4 + (row & 3)) * 4);
}
DI u32x4 kgen_expand(const char* tin, int z) {
    float kv[8];
    if (z < 32) {
#pragma unroll
        for (int e = 0; e < 8; ++e) kv[e] = ktab(tin, e * 32 + z);
    } else if (z < 64) {
        const int r = z - 32;
        const float v0 = ktab(tin, 256 + r), v2 = ktab(tin, 288 + r), v4 = ktab(tin, 320 + r), v6 = ktab(tin, 352 + r), v8 = ktab(tin, z == 63 ? 384 : 257 + r);
        kv[0] = v0; kv[1] = v0; kv[2] = v2; kv[3] = v4; kv[4] = v4; kv[5] = v4; kv[6] = v6; kv[7] = v8;
    } else if (z < 128) {
        const int r = z - 64, r1 = r + 1;
        const int i0 = (12 + 2 * (r >> 5)) * 32 + (r & 31), i8 = (12 + 2 * (r1 >> 5)) * 32 + (r1 & 31);
        const float v0 = ktab(tin, i0), v4 = ktab(tin, i0 + 32), v8 = ktab(tin, i8);
        kv[0] = v0; kv[1] = v0; kv[2] = v0; kv[3] = v4; kv[4] = v4; kv[5] = v4; kv[6] = v8; kv[7] = v8;
    } else {
        const float v0 = ktab(tin, z + 384), v8 = ktab(tin, z + 385);
        kv[0] = v0; kv[1] = v0; kv[2] = v0; kv[3] = v0; kv[4] = (z & 1) ? v8 : v0; kv[5] = v8; kv[6] = v8; kv[7] = v8;
    }
    return (u32x4){pkh(kv[7], kv[6]), pkh(kv[5], kv[4]), pkh(kv[3], kv[2]), pkh(kv[1], kv[0])};
}
struct KRaw { u32 v[11]; };
DI void kgen_load(KRaw& p, int d, int n, const u16* logA, const u16* Aim, const u16* Cre, const u16* Cim, const u16* logdt) {
    const u32 vo = (u32)((d * NST + n) * 2), v1 = (u32)((d * NST + (n ^ 64)) * 2), v2 = (u32)((d * NST + (n ^ 128)) * 2),
              v3 = (u32)((d * NST + (n ^ 192)) * 2), vd = (u32)(d * 2);
    asm volatile("global_load_ushort %0, %11, %16\n\tglobal_load_ushort %1, %11, %17\n\tglobal_load_ushort %2, %11, %18\n\t"
                 "global_load_ushort %3, %11, %19\n\tglobal_load_ushort %4, %15, %20\n\t"
                 "global_load_ushort %5, %12, %18\n\tglobal_load_ushort %6, %12, %19\n\tglobal_load_ushort %7, %13, %18\n\t"
                 "global_load_ushort %8, %13, %19\n\tglobal_load_ushort %9, %14, %18\n\tglobal_load_ushort %10, %14, %19"
                 : "=&v"(p.v[0]), "=&v"(p.v[1]), "=&v"(p.v[2]), "=&v"(p.v[3]), "=&v"(p.v[4]), "=&v"(p.v[5]), "=&v"(p.v[6]), "=&v"(p.v[7]),
                   "=&v"(p.v[8]), "=&v"(p.v[9]), "=&v"(p.v[10])
                 : "v"(vo), "v"(v1), "v"(v2), "v"(v3), "v"(vd), "s"(logA), "s"(Aim), "s"(Cre), "s"(Cim), "s"(logdt)
                 : "memory");
}
template <int N> DI void kgen_wait(KRaw& p) {
    asm volatile("s_waitcnt vmcnt(%11)"
                 : "+v"(p.v[0]), "+v"(p.v[1]), "+v"(p.v[2]), "+v"(p.v[3]), "+v"(p.v[4]), "+v"(p.v[5]), "+v"(p.v[6]), "+v"(p.v[7]), "+v"(p.v[8]),
                   "+v"(p.v[9]), "+v"(p.v[10])
                 : "n"(N) : "memory");
}
DI float kgen_wave_raw(char* stg, int lane, const KRaw& p) {
    float cabs = 0.f;
#pragma unroll
    for (int k = 0; k < 4; ++k) {
        const int ia = k == 0 ? 2 : 3 + 2 * k, ib = k == 0 ? 3 : 4 + 2 * k;
        cabs += __builtin_fabsf(bf1((u16)p.v[ia])) + __builtin_fabsf(bf1((u16)p.v[ib]));
    }
    return kgen_wave(stg, lane, bf1((u16)p.v[0]), bf1((u16)p.v[1]), bf1((u16)p.v[2]), bf1((u16)p.v[3]), bf1((u16)p.v[4]), cabs);
}
DI void soft_bar(char* cnt, int lane, u32 target) {
    wait_lgkm0();
    if (lane == 0) __hip_atomic_fetch_add((LAS u32*)cnt, 1u, __ATOMIC_RELAXED, __HIP_MEMORY_SCOPE_WORKGROUP);
    for (int spin = 0; spin < (1 << 22); ++spin) {
        if (__hip_atomic_load((LAS u32*)cnt, __ATOMIC_RELAXED, __HIP_MEMORY_SCOPE_WORKGROUP) >= target) break;
        asm volatile("s_sleep 1" ::: "memory");
    }
    asm volatile("" ::: "memory");
}

DI void soft_arrive(char* cnt, int lane) {
    wait_lgkm0();
    if (lane == 0) __hip_atomic_fetch_add((LAS u32*)cnt, 1u, __ATOMIC_RELAXED, __HIP_MEMORY_SCOPE_WORKGROUP);
}
DI void soft_wait(char* cnt, u32 target) {
    for (int spin = 0; spin < (1 << 22); ++spin) {
        if (__hip_atomic_load((LAS u32*)cnt, __ATOMIC_RELAXED, __HIP_MEMORY_SCOPE_WORKGROUP) >= target) break;
        asm volatile("s_sleep 1" ::: "memory");
    }
    asm volatile("" ::: "memory");
}

DI void tile_map(int b, int& panel, int& grp) {
    const int xcd = b & 7, idx = b >> 3, y = xcd >> 1, side = xcd & 1;
    if (idx < 24) { panel = 8 * y + (idx & 7); grp = (idx >> 3) + 4 * side; }
    else { panel = 8 * y + 4 * side + (idx - 24); grp = 3; }
}
DI void l2_touch(u32& sink, const char* base, int lane) {
    asm volatile("global_load_dword %0, %1, %2" : "+v"(sink) : "v"(lane * 64), "s"(base) : "memory");
}

constexpr int K1_XSTRIDE = 912;
#define K1R 7
constexpr int K1_RING = K1R;
constexpr int K1_EARLY = 2;
constexpr int K1_OFF_RING = 64 * K1_XSTRIDE;
constexpr int K1_OFF_PS = K1_OFF_RING + 4 * K1_RING * 2048;
constexpr int K1_OFF_ST = K1_OFF_PS + 2048;
constexpr int K1_OFF_CNT = K1_OFF_ST + 256;
constexpr int KG_BYTES = 4 * CV_STG + 2 * 3072;
constexpr int K1_OFF_KG = K1_OFF_CNT + 32;
constexpr int K1_LDS = K1_OFF_KG + KG_BYTES;
static_assert(K1_LDS <= 163840 && 2 * KG_BYTES + 64 <= K1_LDS, "LDS");
DI bool kgen_in_k2(int d) { return d < 32 || (d >= 256 && d < 288); }
DI int kgen_k1_channel(int q) { return q < 224 ? 32 + q : 64 + q; }
DI void kgen_channel(char* kgb, char* cnt, int round, const KRaw& pr, int ft, int lane, int d, char* ws) {
    const int n = ft * 64 + lane;
    char* T = kgb + 4 * CV_STG + (round & 1) * 3072;
    const float kinv = kgen_wave_raw(kgb + ft * CV_STG, lane, pr);
    soft_bar(cnt, lane, 4 * (2 * round + 1));
    kgen_reduce(kgb, T, n);
    soft_bar(cnt, lane, 4 * (2 * round + 2));
    gst16(ws + OFF_KR + (size_t)d * 4096 + n * 16, kgen_expand(T, n));
    if (n == 0) *(float*)(ws + OFF_KI + d * 4) = kinv;
}
constexpr u32 TAG_C = 0x9E3779B9u, TAG_X = 0x5BD1E995u;

__global__ __launch_bounds__(512, 2) void k1_kernel(const u16* x, const u16* w_in, const u16* w_out, const u16* w_sp, const u16* logA,
                                                    const u16* Aim, const u16* Cre, const u16* Cim, const u16* logdt, char* ws) {
    extern __shared__ __attribute__((aligned(16))) char smem[];
    const int tid = threadIdx.x, lane = tid & 63;
    const int wid = __builtin_amdgcn_readfirstlane(tid >> 6);
    const int b = blockIdx.x;
    if (b >= 224) {
        const int a = b - 224, grp = wid >> 2, ftk = wid & 3, nch = 3 - grp, q0 = 224 + 5 * a + 3 * grp;
        char* cnt = smem + grp * 16;
        char* kgb = smem + 64 + grp * KG_BYTES;
        STAMP_DECL;
        STAMP(0, 0);
        KRaw pr[3];
#pragma unroll
        for (int c = 0; c < 3; ++c) kgen_load(pr[c], kgen_k1_channel(q0 + (c < nch ? c : 0)), ftk * 64 + lane, logA, Aim, Cre, Cim, logdt);
        if (tid < 8) *(u32*)(smem + tid * 4) = 0;
        lbar();
        if (grp == 1) {
            const int u = a * 256 + (tid - 256);
            if (u < 28 * 2 * 2 * 56) {
                const int o = u % 56, r = u / 56, h = r & 1, s = (r >> 1) & 1, ft = r >> 2;
                u32x4 rows[8];
#pragma unroll
                for (int j = 0; j < 8; ++j) {
                    const int f = 32 * ft + 16 * s + 8 * (j >> 2) + 4 * h + (j & 3);
                    rows[j] = *(const u32x4*)(w_out + (size_t)f * DM + 8 * o);
                }
                char* wo = ws + OFF_WO;
#pragma unroll
                for (int dd = 0; dd < 8; ++dd) {
                    u32x4 v;
#pragma unroll
                    for (int k = 0; k < 4; ++k) {
                        const u32 ra = rows[2 * k][dd >> 1], rb = rows[2 * k + 1][dd >> 1];
                        v[k] = (dd & 1) ? ((ra >> 16) | (rb & 0xffff0000u)) : ((ra & 0xffffu) | (rb << 16));
                    }
                    const int d = 8 * o + dd, dt = d >> 5, dl = d & 31;
                    gst16(wo + ((size_t)(((dt * 28 + ft) * 2 + s) * 64 + dl + 32 * h)) * 16, v);
                }
            }
        }
#pragma unroll
        for (int c = 0; c < 3; ++c) kgen_wait<0>(pr[c]);
        STAMP(0, 1);
#pragma unroll
        for (int c = 0; c < 3; ++c) {
            if (c < nch) kgen_channel(kgb, cnt, c, pr[c], ftk, lane, kgen_k1_channel(q0 + c), ws);
            if (c == 0) STAMP(0, 2);
            if (c == 1) STAMP(0, 3);
            if (c == 2) STAMP(0, 4);
        }
        STAMP(0, 14);
        return;
    }
    int p, j;
    tile_map(b, p, j);
    const u32 lds0 = (u32)(size_t)smem;
    STAMP_DECL;
    STAMP(0, 0);
    const int ft = wid & 3;
    auto load_x = [&]() LAM {
        if (lane < 56) {
            const char* xb = (const char*)x + (size_t)(64 * p + 8 * wid) * (DM * 2);
#pragma unroll
            for (int i = 0; i < 8; ++i) glds16(xb + i * (DM * 2), lane * 16, lds0 + (8 * wid + i) * K1_XSTRIDE);
        }
    };
    auto center_x = [&]() LAM {
        const int row = 8 * wid + (lane >> 3), t8 = lane & 7;
        char* xp = smem + row * K1_XSTRIDE + t8 * 16;
        float s0 = 0.f, s1 = 0.f, q0 = 0.f, q1 = 0.f;
        u32x4 xr[7];
#pragma unroll
        for (int i = 0; i < 7; ++i) xr[i] = lds16(xp + i * 128);
        sched_fence();
#pragma unroll
        for (int i = 0; i < 7; ++i) {
            const u32x4 v = xr[i];
            s0 = dot2bf(v[0], 0x3F803F80u, s0); s1 = dot2bf(v[1], 0x3F803F80u, s1);
            q0 = dot2bf(v[0], v[0], q0); q1 = dot2bf(v[1], v[1], q1);
            s0 = dot2bf(v[2], 0x3F803F80u, s0); s1 = dot2bf(v[3], 0x3F803F80u, s1);
            q0 = dot2bf(v[2], v[2], q0); q1 = dot2bf(v[3], v[3], q1);
        }
        const float mean = red8(s0 + s1) * (1.0f / DM);
        const float var = __builtin_fmaxf(red8(q0 + q1) * (1.0f / DM) - mean * mean, 0.f);
        const float mb = rbf(mean);
#pragma unroll
        for (int i = 0; i < 7; ++i) {
            u32x4 o;
#pragma unroll
            for (int k = 0; k < 4; ++k) o[k] = pkbf(bflo(xr[i][k]) - mb, bfhi(xr[i][k]) - mb);
            *(u32x4*)(xp + i * 128) = o;
        }
        const float sd = rbf(__builtin_amdgcn_sqrtf(rbf(rbf(var) + 1e-5f)));
        if (t8 == 0) *(float*)(smem + K1_OFF_ST + row * 4) = __builtin_amdgcn_rcpf(sd);
    };

    if (wid >= 4) {
        load_x();
        KRaw pr;
        kgen_load(pr, kgen_k1_channel(b), ft * 64 + lane, logA, Aim, Cre, Cim, logdt);
        bar();
        __builtin_amdgcn_s_setprio(2);
        wait_vm<11>();
        center_x();
        soft_arrive(smem + K1_OFF_CNT + 4, lane);
        kgen_wait<0>(pr);
        __builtin_amdgcn_s_setprio(0);
        kgen_channel(smem + K1_OFF_KG, smem + K1_OFF_CNT + 8, 0, pr, ft, lane, kgen_k1_channel(b), ws);
        {
            u32 sink = 0;
#pragma unroll
            for (int r = 0; r < 3; ++r) {
                int row = (b * 4 + ft) * 3 + r;
                row = row < SL ? row : SL - 1;
                l2_touch(sink, (const char*)w_sp + ((size_t)row << 12), lane);
            }
            asm volatile("s_waitcnt vmcnt(0)" : "+v"(sink)::"memory");
        }
        STAMP(0, 14);
        return;
    }
    u32 epoch;
    {
        const char* ep = ws + OFF_EP + p * 128;
        const u32 zero = 0;
        asm volatile("global_load_dword %0, %1, %2 sc1" : "=&v"(epoch) : "v"(zero), "s"(ep) : "memory");
    }
    load_x();
    const u32 wvoff = (u32)(((lane >> 2) * (2 * FH) + 128 * j + 32 * ft + (lane & 3) * 8) * 2);
    const u32 ring = lds0 + K1_OFF_RING + ft * (K1_RING * 2048);
    auto issue_w1 = [&](auto I, int z1) LAM {
        constexpr int ks = I.value, slot = ks % K1_RING;
        glds16((const char*)w_in + (size_t)ks * 16 * (2 * FH) * 2 + (z1 ? 0 : FH * 2), wvoff, ring + slot * 2048 + z1 * 1024);
    };
    auto issue_w = [&](auto I) LAM { issue_w1(I, 0); issue_w1(I, 1); };
    static_for<0, K1_EARLY>(issue_w);
    if (tid < 8) *(u32*)(smem + K1_OFF_CNT + tid * 4) = 0;
    lbar();
    __builtin_amdgcn_s_setprio(2);
    asm volatile("s_waitcnt vmcnt(%1)" : "+v"(epoch) : "n"(2 * K1_EARLY) : "memory");
    STAMP(0, 1);
    center_x();
    soft_arrive(smem + K1_OFF_CNT + 4, lane);
    STAMP(0, 2);
    static_for<K1_EARLY, K1_RING>(issue_w);
    soft_wait(smem + K1_OFF_CNT + 4, 8);
    __builtin_amdgcn_s_setprio(3);
    STAMP(0, 3);

    f32x16 a2[2], a1[2];
#pragma unroll
    for (int i = 0; i < 16; ++i) { a2[0][i] = 0.f; a2[1][i] = 0.f; a1[0][i] = 0.f; a1[1][i] = 0.f; }
    {
        const int h = lane >> 5, blk = (lane >> 4) & 1, q = (lane & 15) >> 2, pp = lane & 3;
        const char* aptr = smem + K1_OFF_RING + ft * (K1_RING * 2048) + (8 * h + q) * 64 + 32 * blk + 8 * pp;
        const char* bptr = smem + (lane & 31) * K1_XSTRIDE + h * 16;
        u32x4 f2[3], f1[3], b0[3], b1[3];
        auto rd_a = [&](auto I) LAM {
            constexpr int ks = I.value, cur = ks % 3, so = (ks % K1_RING) * 2048;
            const u32x2 l2 = tr_read(aptr + so), h2 = tr_read(aptr + so + 256);
            f2[cur] = (u32x4){l2[0], l2[1], h2[0], h2[1]};
            b0[cur] = lds16(bptr + ks * 32);
        };
        auto rd_b = [&](auto I) LAM {
            constexpr int ks = I.value, cur = ks % 3, so = (ks % K1_RING) * 2048;
            b1[cur] = lds16(bptr + 32 * K1_XSTRIDE + ks * 32);
            const u32x2 l1 = tr_read(aptr + so + 1024), h1 = tr_read(aptr + so + 1280);
            f1[cur] = (u32x4){l1[0], l1[1], h1[0], h1[1]};
        };
        wait_vm<2 * (K1_RING - 1)>();
        rd_a(IC<0>{}); rd_b(IC<0>{});
        wait_vm<2 * (K1_RING - 2)>();
        rd_a(IC<1>{}); rd_b(IC<1>{});
        static_for<0, 28>([&](auto I) LAM {
            constexpr int ks = I.value, cur = ks % 3;
            sched_fence();
            a2[0] = mfma32_bf(f2[cur], b0[cur], a2[0]);
            sched_fence();
            if constexpr (ks + K1_RING < 28) issue_w1(IC<ks + K1_RING>{}, 0);
            sched_fence();
            a2[1] = mfma32_bf(f2[cur], b1[cur], a2[1]);
            sched_fence();
            if constexpr (ks + 2 < 28) {
                constexpr int issued = (ks + K1_RING - 1 < 27) ? ks + K1_RING - 1 : 27;
                constexpr int extra = (ks + K1_RING < 28) ? 1 : 0;
                wait_vm<2 * (issued - (ks + 2)) + extra>();
                rd_a(IC<ks + 2>{});
            }
            sched_fence();
            a1[0] = mfma32_bf(f1[cur], b0[cur], a1[0]);
            sched_fence();
            if constexpr (ks + K1_RING < 28) issue_w1(IC<ks + K1_RING>{}, 1);
            sched_fence();
            a1[1] = mfma32_bf(f1[cur], b1[cur], a1[1]);
            sched_fence();
            if constexpr (ks + 2 < 28) rd_b(IC<ks + 2>{});
        });
    }
    STAMP(0, 4);

    asm volatile("s_nop 7\n\ts_nop 7" : "+v"(a2[0]), "+v"(a2[1]), "+v"(a1[0]), "+v"(a1[1]));
    const int h = lane >> 5;
    float ri[2];
#pragma unroll
    for (int st = 0; st < 2; ++st) ri[st] = *(const float*)(smem + K1_OFF_ST + (32 * st + (lane & 31)) * 4);
    auto fold = [&](f32x16 (&acc)[2], int) LAM {
#pragma unroll
        for (int st = 0; st < 2; ++st)
#pragma unroll
            for (int i = 0; i < 16; ++i) acc[st][i] = smul(acc[st][i], ri[st]);
    };
    const int ftile = 4 * j + ft;
    fold(a2, 0);
    u32 zp[2][8];
    {
        float mw[2], m2[2];
#pragma unroll
        for (int st = 0; st < 2; ++st) {
            float s0 = 0.f, q0 = 0.f;
#pragma unroll
            for (int i = 0; i < 8; i += 2) {
                const u32 c = pkbf(a2[st][2 * i], a2[st][2 * i + 1]), c2 = pkbf(a2[st][2 * i + 2], a2[st][2 * i + 3]);
                float g0 = bflo(c), g1 = bfhi(c), g2 = bflo(c2), g3 = bfhi(c2);
                gelu4(g0, g1, g2, g3);
                zp[st][i] = pkbf(g0, g1); zp[st][i + 1] = pkbf(g2, g3);
                s0 = dot2bf(zp[st][i], 0x3F803F80u, s0); q0 = dot2bf(zp[st][i], zp[st][i], q0);
                s0 = dot2bf(zp[st][i + 1], 0x3F803F80u, s0); q0 = dot2bf(zp[st][i + 1], zp[st][i + 1], q0);
            }
            const float sum = addx32(s0), sq = addx32(q0);
            mw[st] = sum * (1.0f / 32.0f);
            m2[st] = __builtin_fmaxf(sq - sum * mw[st], 0.f);
        }
        f32x2 pv = {h ? mw[1] : mw[0], h ? m2[1] : m2[0]};
        *(f32x2*)(smem + K1_OFF_PS + (ft * 64 + lane) * 8) = pv;
    }
    STAMP(0, 5);
    const u32 tagA = epoch + TAG_C, tagB = tagA ^ TAG_X;
    char* gr = ws + OFF_GR + (size_t)p * 7 * 1024;
    const __amdgpu_buffer_rsrc_t grs = __builtin_amdgcn_make_buffer_rsrc(gr, 0, 7 * 1024, 0x00020000);
    {
        wait_lgkm0();
        u32 old = 0;
        if (lane == 0) old = __hip_atomic_fetch_add((LAS u32*)(smem + K1_OFF_CNT), 1u, __ATOMIC_RELAXED, __HIP_MEMORY_SCOPE_WORKGROUP);
        old = __builtin_amdgcn_readfirstlane(old);
        if (old == 3) {
            f32x2 a[4];
#pragma unroll
            for (int w = 0; w < 4; ++w) a[w] = *(const f32x2*)(smem + K1_OFF_PS + (w * 64 + lane) * 8);
            const float mean = (a[0][0] + a[1][0] + a[2][0] + a[3][0]) * 0.25f;
            float m2 = a[0][1] + a[1][1] + a[2][1] + a[3][1];
#pragma unroll
            for (int w = 0; w < 4; ++w) { const float dlt = a[w][0] - mean; m2 = __builtin_fmaf(32.0f * dlt, dlt, m2); }
            u32x4 g = {__builtin_bit_cast(u32, mean), tagA, __builtin_bit_cast(u32, m2), tagB};
            __builtin_amdgcn_raw_buffer_store_b128(g, grs, j * 1024 + lane * 16, 0, 16);
        }
    }
    __builtin_amdgcn_s_setprio(1);
    STAMP(0, 6);
    u32x4 g[7];
    fold(a1, 1);
    {
        char* z1 = ws + OFF_Z1;
#pragma unroll
        for (int st = 0; st < 2; ++st)
#pragma unroll
            for (int hf = 0; hf < 2; ++hf) {
                if (st == 1 && hf == 1) {
                    sched_fence();
#pragma unroll
                    for (int w = 0; w < 7; ++w) g[w] = __builtin_amdgcn_raw_buffer_load_b128(grs, w * 1024 + lane * 16, 0, 16);
                    sched_fence();
                }
                u32x4 v;
#pragma unroll
                for (int k = 0; k < 4; k += 2) {
                    const u32 c = pkbf(a1[st][8 * hf + 2 * k], a1[st][8 * hf + 2 * k + 1]), c2 = pkbf(a1[st][8 * hf + 2 * k + 2], a1[st][8 * hf + 2 * k + 3]);
                    float g0 = bflo(c), g1 = bfhi(c), g2 = bflo(c2), g3 = bfhi(c2);
                    gelu4(g0, g1, g2, g3);
                    v[k] = pkbf(g0, g1); v[k + 1] = pkbf(g2, g3);
                }
                gst16(z1 + ((size_t)((ftile * 64 + 2 * p + st) * 2 + hf) * 64 + lane) * 16, v);
            }
    }
    {
        u16* xnt = (u16*)(ws + OFF_XNT);
        const int g = lane >> 4, q = (lane & 15) >> 2, pp = lane & 3, i16 = lane & 15;
#pragma unroll
        for (int sh = 0; sh < 2; ++sh) {
            const int dbase = 64 * j + 16 * ft;
            const char* a = smem + (32 * sh + 8 * g + q) * K1_XSTRIDE + (dbase + 4 * pp) * 2;
            const u32x2 lo = tr_read(a), hi = tr_read(a + 4 * K1_XSTRIDE);
            const u32 raw[4] = {lo[0], lo[1], hi[0], hi[1]};
            const f32x4 r0 = *(const f32x4*)(smem + K1_OFF_ST + (32 * sh + 8 * g) * 4), r1 = *(const f32x4*)(smem + K1_OFF_ST + (32 * sh + 8 * g + 4) * 4);
            u32x4 o;
#pragma unroll
            for (int k = 0; k < 4; ++k) {
                const float ra = k < 2 ? r0[2 * k] : r1[2 * k - 4], rb = k < 2 ? r0[2 * k + 1] : r1[2 * k - 3];
                o[k] = pkbf(bflo(raw[k]) * ra, bfhi(raw[k]) * rb);
            }
            gst16(xnt + (size_t)(dbase + i16) * SL + 64 * p + 32 * sh + 8 * g, o);
        }
    }
    STAMP(0, 7);
    __builtin_amdgcn_s_setprio(3);
    float mb, rinv;
    {
        for (int spin = 0; spin < (1 << 22); ++spin) {
            u32 bad = 0;
#pragma unroll
            for (int w = 0; w < 7; ++w) bad |= (g[w][1] ^ tagA) | (g[w][3] ^ tagB);
            if (__all(bad == 0)) break;
            asm volatile("" ::: "memory");
#pragma unroll
            for (int w = 0; w < 7; ++w) g[w] = __builtin_amdgcn_raw_buffer_load_b128(grs, w * 1024 + lane * 16, 0, 16);
        }
        float mean = 0.f, m2 = 0.f;
#pragma unroll
        for (int w = 0; w < 7; ++w) { mean += __builtin_bit_cast(float, (u32)g[w][0]); m2 += __builtin_bit_cast(float, (u32)g[w][2]); }
        mean *= (1.0f / 7.0f);
#pragma unroll
        for (int w = 0; w < 7; ++w) { const float dlt = __builtin_bit_cast(float, (u32)g[w][0]) - mean; m2 = __builtin_fmaf(128.0f * dlt, dlt, m2); }
        mb = rbf(mean);
        const float vb = rbf(m2 * (1.0f / FH));
        rinv = __builtin_amdgcn_rcpf(rbf(__builtin_amdgcn_sqrtf(rbf(vb + 1e-5f))));
        if (j == 0 && ft == 0 && lane == 0)
            __hip_atomic_store((u32*)(ws + OFF_EP + p * 128), tagA, __ATOMIC_RELAXED, __HIP_MEMORY_SCOPE_AGENT);
    }
    STAMP(0, 8);
    {
        char* tb = smem + K1_OFF_RING + ft * (K1_RING * 2048);
        u32 mbb = __builtin_bit_cast(u32, mb), rib = __builtin_bit_cast(u32, rinv);
        auto sm = __builtin_amdgcn_permlane32_swap(mbb, mbb, false, false);
        auto sr = __builtin_amdgcn_permlane32_swap(rib, rib, false, false);
#pragma unroll
        for (int st = 0; st < 2; ++st) {
            const float m = __builtin_bit_cast(float, (u32)sm[st]), r = __builtin_bit_cast(float, (u32)sr[st]);
#pragma unroll
            for (int g = 0; g < 4; ++g) {
                u32x2 v;
#pragma unroll
                for (int k = 0; k < 2; ++k) {
                    const u32 zz = zp[st][2 * g + k];
                    const u32 c = pkbf(bflo(zz) - m, bfhi(zz) - m);
                    v[k] = pkbf(bflo(c) * r, bfhi(c) * r);
                }
                *(u32x2*)(tb + (32 * st + (lane & 31)) * 72 + (8 * g + 4 * h) * 2) = v;
            }
        }
        const int blk = (lane >> 4) & 1, q = (lane & 15) >> 2, pp = lane & 3;
        char* z2t = ws + OFF_Z2T;
#pragma unroll
        for (int ks = 0; ks < 4; ++ks) {
            const char* a = tb + (16 * ks + 8 * h + q) * 72 + 32 * blk + 8 * pp;
            const u32x2 lo = tr_read(a), hi = tr_read(a + 4 * 72);
            gst16(z2t + ((size_t)(ftile * 128 + 4 * p + ks) * 64 + lane) * 16, (u32x4){lo[0], lo[1], hi[0], hi[1]});
        }
    }
    STAMP(0, 14);
}

constexpr int CV_CS = 322 * 16;
constexpr int CV_OFF_KR = 8 * CV_CS;
constexpr int K2_LDS = 6 * 8192;
constexpr int CV_OFF_PB = 0;
constexpr int G2_SLOTS = 6;
constexpr int G2_ZD = 5;
constexpr int g2_ops(int i) { return (i + G2_SLOTS - 1 < 32 ? 2 : 0) + (i + G2_ZD < 32 ? 4 : 0) + (i == 27 ? 6 : 0); }
constexpr int g2_younger(int c) {
    int n = 0;
    const int wi = (c + 1) - (G2_SLOTS - 1);
    const int zi = c - G2_ZD;
    const bool need_w = c + 1 < 32;
    if (need_w && wi >= 0 && wi > zi) {
        n = g2_ops(wi) - 2;
        for (int i = wi + 1; i < c; ++i) n += g2_ops(i);
        return n;
    }
    if (zi >= 0 && (!need_w || zi >= wi)) {
        n = (zi == 27 ? 6 : 0);
        for (int i = zi + 1; i < c; ++i) n += g2_ops(i);
        return n;
    }
    const int lastp = need_w ? 6 * (c + 1) + 1 : 6 * c + 5;
    n = 6 * G2_ZD - 1 - lastp;
    for (int i = 0; i < c; ++i) n += g2_ops(i);
    return n;
}
static_assert(G2_SLOTS - 1 == G2_ZD, "prologue assumes equal depth");

#define KM(c) (c)
DI void gemm2_role(int b, char* smem, const u16* w_sp, const u16* b_sp, char* ws) {
    const int tid = threadIdx.x, lane = tid & 63;
    const int wid = __builtin_amdgcn_readfirstlane(tid >> 6);
    int tp, j;
    tile_map(b, tp, j);
    const int ftile = 4 * j + wid;
    const u32 lds0 = (u32)(size_t)smem;
    STAMP_DECL;
    STAMP(1, 0);
    u32 wvoff[2];
#pragma unroll
    for (int i = 0; i < 2; ++i) {
        const int r = 16 * wid + 8 * i + (lane >> 3), cc = (lane & 7) ^ ((r >> 1) & 7);
        wvoff[i] = (u32)(((64 * tp + r) * SL + cc * 8) * 2);
    }
    const char* z2base = ws + OFF_Z2T + (size_t)ftile * 128 * 1024;
    const u32 zvoff = lane * 16;
    u32x4 zf[G2_ZD][4];
    u32x4 z1v[2][2];
    u32 bsv[2];
    auto issue_w1 = [&](auto C, int i) LAM {
        constexpr int c = C.value, slot = c % G2_SLOTS;
        glds16((const char*)w_sp + KM(c) * 128, wvoff[i], lds0 + slot * 8192 + wid * 2048 + i * 1024);
    };
    static_for<0, G2_ZD>([&](auto C) LAM {
        constexpr int c = C.value;
        issue_w1(C, 0); issue_w1(C, 1);
        gload4x(zf[c][0], zf[c][1], zf[c][2], zf[c][3], zvoff, z2base + KM(c) * 4096);
    });
    f32x16 acc[2];
#pragma unroll
    for (int i = 0; i < 16; ++i) { acc[0][i] = 0.f; acc[1][i] = 0.f; }
    const int h = lane >> 5, tl = lane & 31;
    u32 roff[2][4];
#pragma unroll
    for (int tt = 0; tt < 2; ++tt)
#pragma unroll
        for (int kk = 0; kk < 4; ++kk) { const int r = 32 * tt + tl; roff[tt][kk] = r * 128 + (((2 * kk + h) ^ ((r >> 1) & 7)) * 16); }
    u32x4 wa[2][2], wb[2][2];
    auto read_w = [&](u32x4 (&dst)[2][2], int slot, int kk0) LAM {
#pragma unroll
        for (int kk = 0; kk < 2; ++kk)
#pragma unroll
            for (int tt = 0; tt < 2; ++tt) dst[kk][tt] = lds16(smem + slot * 8192 + roff[tt][kk0 + kk]);
    };
    wait_vm<6 * G2_ZD - 2>();
    bar();
    read_w(wa, 0, 0);
    STAMP(1, 1);
    static_for<0, 32>([&](auto C) LAM {
        constexpr int c = C.value, set = c % G2_ZD;
        wait_vm_tie4<g2_younger(c)>(zf[set][0], zf[set][1], zf[set][2], zf[set][3]);
        if constexpr (c == 8) STAMP(1, 2);
        if constexpr (c == 16) STAMP(1, 3);
        if constexpr (c == 24) STAMP(1, 4);
        lbar();
        constexpr bool more_w = c + G2_SLOTS - 1 < 32;
        constexpr bool more_z = c + G2_ZD < 32;
        const char* zn = z2base + KM(c + G2_ZD) * 4096;
        if constexpr (more_w) { issue_w1(IC<c + G2_SLOTS - 1>{}, 0); issue_w1(IC<c + G2_SLOTS - 1>{}, 1); }
        read_w(wb, c % G2_SLOTS, 2);
        sched_fence();
#define G2M(kk, tt, W) acc[tt] = mfma32_bf(zf[set][kk], W, acc[tt])
        G2M(0, 0, wa[0][0]);
        G2M(0, 1, wa[0][1]);
        sched_fence();
        if constexpr (more_z) gload1(zf[set][0], zvoff, zn);
        G2M(1, 0, wa[1][0]);
        G2M(1, 1, wa[1][1]);
        sched_fence();
        if constexpr (more_z) gload1(zf[set][1], zvoff, zn + 1024);
        if constexpr (c + 1 < 32) read_w(wa, (c + 1) % G2_SLOTS, 0);
        sched_fence();
        G2M(2, 0, wb[0][0]);
        G2M(2, 1, wb[0][1]);
        G2M(3, 0, wb[1][0]);
        G2M(3, 1, wb[1][1]);
        sched_fence();
        if constexpr (more_z) { gload1(zf[set][2], zvoff, zn + 2048); gload1(zf[set][3], zvoff, zn + 3072); }
        if constexpr (c == 27) {
            const char* z1 = ws + OFF_Z1 + (size_t)(ftile * 64 + 2 * tp) * 2048;
            gload4x(z1v[0][0], z1v[0][1], z1v[1][0], z1v[1][1], zvoff, z1);
            gload_u16x2(bsv[0], bsv[1], (u32)((64 * tp + (lane & 31)) * 2), (const char*)b_sp);
        }
    });
    STAMP(1, 5);
    asm volatile("s_waitcnt vmcnt(0)\n\ts_nop 7\n\ts_nop 7" : "+v"(acc[0]), "+v"(acc[1]), "+v"(z1v[0][0]), "+v"(z1v[0][1]), "+v"(z1v[1][0]), "+v"(z1v[1][1]), "+v"(bsv[0]), "+v"(bsv[1]));
    char* ht = ws + OFF_HT;
#pragma unroll
    for (int tt = 0; tt < 2; ++tt) {
        const float bs = bf1((u16)bsv[tt]);
#pragma unroll
        for (int s = 0; s < 2; ++s) {
            u32x4 v;
#pragma unroll
            for (int k = 0; k < 4; ++k) {
                const u32 g = pkbf(acc[tt][8 * s + 2 * k], acc[tt][8 * s + 2 * k + 1]);
                const u32 g2 = pkbf(bflo(g) + bs, bfhi(g) + bs);
                const u32 z = z1v[tt][s][k];
                v[k] = pkbf(bflo(z) * bflo(g2), bfhi(z) * bfhi(g2));
            }
            gst16(ht + ((size_t)(((2 * tp + tt) * 28 + ftile) * 2 + s) * 64 + lane) * 16, v);
        }
    }
    STAMP(1, 14);
}

template <int NS> DI void conv_seg(const char* smem, const char* kr, int lane, int tt, int e0, f32x4 (&acc)[4]) {
    constexpr int PD = 4;
    const int q = lane & 15, kg = lane >> 4, c = lane & 7, pl = (lane >> 3) & 1;
    const int m0 = -(32 * tt + 16) + e0;
    const char* ap = kr + (q - kg + 64) * 16 - 64 * m0 - 64 * (NS - 1);
    const char* bp = smem + c * CV_CS + (64 * (2 * tt + pl) + kg + 64) * 16 + 64 * m0;
    u32x4 F[NS + 12], B[NS];
#pragma unroll
    for (int n = 0; n < 12 + PD; ++n) F[n] = lds16(ap + 64 * (NS + 11 - n));
#pragma unroll
    for (int e = 0; e < PD; ++e) B[e] = lds16(bp + 64 * e);
#pragma unroll
    for (int a = 0; a < 4; ++a) acc[a] = (f32x4){0.f, 0.f, 0.f, 0.f};
    static_for<0, NS>([&](auto E) LAM {
        constexpr int e = E.value;
        if constexpr (e + PD < NS) { F[e + 12 + PD] = lds16(ap + 64 * (NS + 11 - (e + 12 + PD))); B[e + PD] = lds16(bp + 64 * (e + PD)); }
        sched_fence();
#pragma unroll
        for (int a = 0; a < 4; ++a) acc[a] = mfma16_h(F[e + 12 - 4 * a], B[e], acc[a]);
        sched_fence();
    });
}

DI void conv_ucopies(char* smem, int tid, u32x4 uprev, u32x4 ucur) {
    u32 v[8];
#pragma unroll
    for (int k = 0; k < 4; ++k) { v[k] = pkh(bflo(uprev[k]), bfhi(uprev[k])); v[4 + k] = pkh(bflo(ucur[k]), bfhi(ucur[k])); }
#pragma unroll
    for (int c = 0; c < 8; ++c) {
        u32x4 o;
#pragma unroll
        for (int k = 0; k < 4; ++k) {
            const int e = c + 1 + 2 * k;
            o[k] = (e & 1) ? __builtin_amdgcn_alignbyte(v[(e + 1) >> 1 < 8 ? (e + 1) >> 1 : 7], v[e >> 1], 2) : v[e >> 1];
        }
        *(u32x4*)(smem + c * CV_CS + (tid + 64) * 16) = o;
    }
}
DI void conv_run(const char* smem, const char* kr, int wid, int lane, f32x4 (&ca)[4], f32x4 (&cb)[4]) {
    if (wid == 0) conv_seg<24>(smem, kr, lane, 1, 0, ca);
    else if (wid == 1) conv_seg<24>(smem, kr, lane, 1, 24, ca);
    else if (wid == 2) { conv_seg<16>(smem, kr, lane, 1, 48, ca); conv_seg<8>(smem, kr, lane, 0, 0, cb); }
    else conv_seg<24>(smem, kr, lane, 0, 8, ca);
}
DI void conv_put(char* smem, int wid, int lane, const f32x4 (&ca)[4], const f32x4 (&cb)[4]) {
    const int kg = lane >> 4, c = lane & 7, pl = (lane >> 3) & 1;
    float* pb = (float*)(smem + CV_OFF_PB) + 512 * pl + c + 32 * kg;
    float* p0 = pb + 1024 * (wid == 3 ? 4 : wid);
#pragma unroll
    for (int a = 0; a < 4; ++a)
#pragma unroll
        for (int r = 0; r < 4; ++r) p0[128 * a + 8 * r] = ca[a][r];
    if (wid == 2) {
#pragma unroll
        for (int a = 0; a < 4; ++a)
#pragma unroll
            for (int r = 0; r < 4; ++r) pb[3 * 1024 + 128 * a + 8 * r] = cb[a][r];
    }
}
DI void conv_out(const char* smem, int tid, int d, float kinv, float dsk, u32x4 ucur, char* ws) {
    const char* yb = smem + CV_OFF_PB + ((8 * tid) & 1023) * 4;
    f32x4 y0, y1;
    if (tid < 128) {
        y0 = *(const f32x4*)(yb + 3 * 4096) + *(const f32x4*)(yb + 4 * 4096);
        y1 = *(const f32x4*)(yb + 3 * 4096 + 16) + *(const f32x4*)(yb + 4 * 4096 + 16);
    } else {
        y0 = (*(const f32x4*)(yb) + *(const f32x4*)(yb + 4096)) + *(const f32x4*)(yb + 2 * 4096);
        y1 = (*(const f32x4*)(yb + 16) + *(const f32x4*)(yb + 4096 + 16)) + *(const f32x4*)(yb + 2 * 4096 + 16);
    }
    u32x4 o;
#pragma unroll
    for (int k = 0; k < 4; ++k) {
        const float ya = k < 2 ? y0[2 * k] : y1[2 * k - 4], yb2 = k < 2 ? y0[2 * k + 1] : y1[2 * k - 3];
        const u32 y = pkbf(ya * kinv, yb2 * kinv);
        const u32 sk = pkbf(bflo(ucur[k]) * dsk, bfhi(ucur[k]) * dsk);
        o[k] = pkbf(bflo(y) + bflo(sk), bfhi(y) + bfhi(sk));
    }
    gst16((u16*)(ws + OFF_ST) + ((size_t)(tid >> 3) * DM + d) * 64 + (tid & 7) * 8, o);
}

template <bool GEN>
DI void conv_role(int d, char* smem, const u16* logA, const u16* Aim, const u16* Cre, const u16* Cim, const u16* logdt, const u16* Dsk,
                  char* ws) {
    const int tid = threadIdx.x, lane = tid & 63;
    const int wid = __builtin_amdgcn_readfirstlane(tid >> 6);
    STAMP_DECL;
    STAMP(1, 0);
    const u32 lds0 = (u32)(size_t)smem;
    KRaw pr;
    if constexpr (GEN) kgen_load(pr, d, tid, logA, Aim, Cre, Cim, logdt);
    else glds16(ws + OFF_KR + (size_t)d * 4096 + wid * 1024, lane * 16, lds0 + CV_OFF_KR + (64 + 64 * wid) * 16);
    const u16* xnt = (const u16*)(ws + OFF_XNT) + (size_t)d * SL;
    u32x4 ucur, uprev;
    u32 dskr, kir;
    {
        const u32 vo = tid * 16, vp = tid > 0 ? tid * 16 - 16 : 0;
        asm volatile("global_load_dwordx4 %0, %4, %6\n\tglobal_load_dwordx4 %1, %5, %6\n\tglobal_load_ushort %2, %7, %8\n\tglobal_load_dword %3, %7, %9"
                     : "=&v"(ucur), "=&v"(uprev), "=&v"(dskr), "=&v"(kir)
                     : "v"(vo), "v"(vp), "s"(xnt), "v"(0u), "s"(Dsk + d), "s"(ws + OFF_KI + d * 4)
                     : "memory");
    }
    if (tid < 64) *(u32x4*)(smem + CV_OFF_KR + tid * 16) = (u32x4){0u, 0u, 0u, 0u};
    float kinv = 0.f;
    if constexpr (GEN) {
        kgen_wait<4>(pr);
        kinv = kgen_wave_raw(smem + wid * CV_STG, lane, pr);
        lbar();
        kgen_reduce(smem, smem + 4 * CV_STG, tid);
        lbar();
        *(u32x4*)(smem + CV_OFF_KR + (tid + 64) * 16) = kgen_expand(smem + 4 * CV_STG, tid);
        lbar();
    }
#pragma unroll
    for (int i = 0; i < 2; ++i) {
        const int zc = tid + 256 * i;
        *(u32x4*)(smem + (zc >> 6) * CV_CS + (zc & 63) * 16) = (u32x4){0u, 0u, 0u, 0u};
    }
    asm volatile("s_waitcnt vmcnt(0)" : "+v"(ucur), "+v"(uprev), "+v"(dskr), "+v"(kir)::"memory");
    if constexpr (!GEN) kinv = __builtin_bit_cast(float, kir);
    if (tid == 0) uprev = (u32x4){0u, 0u, 0u, 0u};
    const float dsk = bf1((u16)dskr);
    conv_ucopies(smem, tid, uprev, ucur);
    STAMP(1, 3);
    lbar();
    STAMP(1, 4);
    f32x4 ca[4], cb[4];
    conv_run(smem, smem + CV_OFF_KR, wid, lane, ca, cb);
    STAMP(1, 5);
    lbar();
    conv_put(smem, wid, lane, ca, cb);
    lbar();
    STAMP(1, 6);
    conv_out(smem, tid, d, kinv, dsk, ucur, ws);
    STAMP(1, 14);
}

__global__ __launch_bounds__(256, 3) void k2_kernel(const u16* w_sp, const u16* b_sp, const u16* logA, const u16* Aim, const u16* Cre,
                                                    const u16* Cim, const u16* logdt, const u16* Dsk, char* ws) {
    extern __shared__ __attribute__((aligned(16))) char smem[];
    const int b = blockIdx.x;
    if (b < 224) gemm2_role(b, smem, w_sp, b_sp, ws);
    if (b >= 224) {
        const int d = b - 224;
        if (kgen_in_k2(d)) conv_role<true>(d, smem, logA, Aim, Cre, Cim, logdt, Dsk, ws);
        else conv_role<false>(d, smem, logA, Aim, Cre, Cim, logdt, Dsk, ws);
    }
}

constexpr int K3_ROW = 272;
constexpr int K3_OFF_S = 4 * 64 * K3_ROW;
constexpr int K3_LDS = K3_OFF_S + 8192;

__global__ __launch_bounds__(256, 1) void k3_kernel(const u16* x, char* ws, u16* out) {
    extern __shared__ __attribute__((aligned(16))) char smem[];
    const int tid = threadIdx.x, lane = tid & 63;
    const int wid = __builtin_amdgcn_readfirstlane(tid >> 6);
    const int b = blockIdx.x, xcd = b & 7, idx = b >> 3;
    const int tp = xcd * 4 + idx / 7, dg = idx % 7;
    STAMP_DECL;
    STAMP(2, 0);
    const char* wo = ws + OFF_WO + lane * 16;
    const char* ht = ws + OFF_HT + lane * 16;
    u32x4 af[14][2], bfv[14][2];
#pragma unroll
    for (int k = 0; k < 14; ++k) {
        const int ft = 7 * wid + (k >> 1), s = k & 1;
#pragma unroll
        for (int a = 0; a < 2; ++a) {
            af[k][a] = *(const u32x4*)(wo + (size_t)(((2 * dg + a) * 28 + ft) * 2 + s) * 1024);
            bfv[k][a] = *(const u32x4*)(ht + (size_t)(((2 * tp + a) * 28 + ft) * 2 + s) * 1024);
        }
    }
    u32x4 xv[2], sv[2];
    const int tl = 16 * wid + (lane & 15);
#pragma unroll
    for (int i = 0; i < 2; ++i) {
        xv[i] = *(const u32x4*)(x + (size_t)(64 * tp + tl) * DM + 64 * dg + 8 * (4 * i + (lane >> 4)));
        sv[i] = *(const u32x4*)(ws + OFF_ST + ((size_t)tp * DM + 64 * dg) * 128 + (tid + 256 * i) * 16);
    }
    sched_fence();
    STAMP(2, 1);
    f32x16 acc[2][2];
#pragma unroll
    for (int a = 0; a < 2; ++a)
#pragma unroll
        for (int c = 0; c < 2; ++c)
#pragma unroll
            for (int i = 0; i < 16; ++i) acc[a][c][i] = 0.f;
#pragma unroll
    for (int k = 0; k < 14; ++k)
#pragma unroll
        for (int a = 0; a < 2; ++a)
#pragma unroll
            for (int c = 0; c < 2; ++c) acc[a][c] = mfma32_bf(af[k][a], bfv[k][c], acc[a][c]);
    STAMP(2, 2);
    {
        const int t32 = lane & 31, h = lane >> 5;
        char* pw = smem + wid * (64 * K3_ROW);
#pragma unroll
        for (int a = 0; a < 2; ++a)
#pragma unroll
            for (int c = 0; c < 2; ++c)
#pragma unroll
                for (int g = 0; g < 4; ++g)
                    *(f32x4*)(pw + (32 * c + t32) * K3_ROW + (32 * a + 8 * g + 4 * h) * 4) =
                        (f32x4){acc[a][c][4 * g], acc[a][c][4 * g + 1], acc[a][c][4 * g + 2], acc[a][c][4 * g + 3]};
#pragma unroll
        for (int i = 0; i < 2; ++i) *(u32x4*)(smem + K3_OFF_S + (tid + 256 * i) * 16) = sv[i];
    }
    STAMP(2, 3);
    __syncthreads();
    STAMP(2, 4);
#pragma unroll
    for (int i = 0; i < 2; ++i) {
        const int o = 4 * i + (lane >> 4);
        f32x4 s0 = {0.f, 0.f, 0.f, 0.f}, s1 = {0.f, 0.f, 0.f, 0.f};
#pragma unroll
        for (int w = 0; w < 4; ++w) {
            const char* pr = smem + w * (64 * K3_ROW) + tl * K3_ROW + o * 32;
            s0 += *(const f32x4*)pr; s1 += *(const f32x4*)(pr + 16);
        }
        const char* sa = smem + K3_OFF_S + (8 * o + ((lane & 15) >> 2)) * 128 + (16 * wid + 4 * (lane & 3)) * 2;
        const u32x2 slo = tr_read(sa), shi = tr_read(sa + 4 * 128);
        const u32 sw[4] = {slo[0], slo[1], shi[0], shi[1]};
        u32x4 ov;
#pragma unroll
        for (int k = 0; k < 4; ++k) {
            const float oa = k < 2 ? s0[2 * k] : s1[2 * k - 4], ob = k < 2 ? s0[2 * k + 1] : s1[2 * k - 3];
            const u32 ob16 = pkbf(oa, ob);
            const float va = bflo(ob16), vb = bfhi(ob16);
            const float ga = va * __builtin_amdgcn_rcpf(1.0f + __builtin_amdgcn_exp2f(-1.4426950408889634f * va));
            const float gb = vb * __builtin_amdgcn_rcpf(1.0f + __builtin_amdgcn_exp2f(-1.4426950408889634f * vb));
            const u32 g = pkbf(ga, gb);
            const u32 pr = pkbf(bflo(sw[k]) * bflo(g), bfhi(sw[k]) * bfhi(g));
            ov[k] = pkbf(bflo(xv[i][k]) + bflo(pr), bfhi(xv[i][k]) + bfhi(pr));
        }
        gst16(out + (size_t)(64 * tp + tl) * DM + 64 * dg + 8 * o, ov);
    }
    STAMP(2, 14);
}
extern "C" void kernel_launch(void* const* d_in, const int* in_sizes, int n_in, void* d_out, int out_size, void* d_ws, size_t ws_size,
                              hipStream_t stream) {
    const u16* x = (const u16*)d_in[0];
    const u16* logA = (const u16*)d_in[1];
    const u16* Aim = (const u16*)d_in[2];
    const u16* Cre = (const u16*)d_in[3];
    const u16* Cim = (const u16*)d_in[4];
    const u16* logdt = (const u16*)d_in[5];
    const u16* Dsk = (const u16*)d_in[6];
    const u16* w_in = (const u16*)d_in[8];
    const u16* w_sp = (const u16*)d_in[9];
    const u16* b_sp = (const u16*)d_in[10];
    const u16* w_out = (const u16*)d_in[11];
    char* ws = (char*)d_ws;
    hipFuncSetAttribute((const void*)k1_kernel, hipFuncAttributeMaxDynamicSharedMemorySize, K1_LDS);
    hipFuncSetAttribute((const void*)k2_kernel, hipFuncAttributeMaxDynamicSharedMemorySize, K2_LDS);
    hipFuncSetAttribute((const void*)k3_kernel, hipFuncAttributeMaxDynamicSharedMemorySize, K3_LDS);
    k1_kernel<<<256, 512, K1_LDS, stream>>>(x, w_in, w_out, w_sp, logA, Aim, Cre, Cim, logdt, ws);
    k2_kernel<<<224 + 448, 256, K2_LDS, stream>>>(w_sp, b_sp, logA, Aim, Cre, Cim, logdt, Dsk, ws);
    k3_kernel<<<224, 256, K3_LDS, stream>>>(x, ws, (u16*)d_out);
}
```

```cpp
#define LAM __attribute__((always_inline))
#include <hip/hip_runtime.h>
#include <hip/hip_bf16.h>
#include <stdint.h>

typedef unsigned short u16;
typedef unsigned int u32;
typedef unsigned long long u64;
#define LAS __attribute__((address_space(3)))
#define DI __device__ __forceinline__
typedef __attribute__((ext_vector_type(2))) __bf16 bf16x2;
typedef __attribute__((ext_vector_type(8))) __bf16 bf16x8;
typedef __attribute__((ext_vector_type(2))) _Float16 f16x2;
typedef __attribute__((ext_vector_type(8))) _Float16 f16x8;
typedef __attribute__((ext_vector_type(2))) float f32x2;
typedef __attribute__((ext_vector_type(4))) float f32x4;
typedef __attribute__((ext_vector_type(16))) float f32x16;
typedef __attribute__((ext_vector_type(4))) short s16x4;
typedef __attribute__((ext_vector_type(2))) u32 u32x2;
typedef __attribute__((ext_vector_type(4))) u32 u32x4;

constexpr int SL = 2048;
constexpr int DM = 448;
constexpr int FH = 896;
constexpr int NST = 256;

constexpr size_t OFF_XNT = 0;
constexpr size_t OFF_Z1 = OFF_XNT + (size_t)DM * SL * 2;
constexpr size_t OFF_Z2T = OFF_Z1 + (size_t)SL * FH * 2;
constexpr size_t OFF_HT = OFF_Z2T + (size_t)SL * FH * 2;
constexpr size_t OFF_WO = OFF_HT + (size_t)SL * FH * 2;
constexpr size_t OFF_ST = OFF_WO + (size_t)FH * DM * 2;
constexpr size_t OFF_GR = OFF_ST + (size_t)DM * SL * 2;
constexpr size_t OFF_EP = OFF_GR + (size_t)32 * 7 * 64 * 16;
constexpr size_t OFF_KR = OFF_EP + 32 * 128;
constexpr size_t OFF_KI = OFF_KR + (size_t)DM * 4096;
constexpr size_t OFF_FL = OFF_KI + 2048;
constexpr int FL_S = 1024;
constexpr int FL_WORDS = FL_S + 448;

#define STAMP(kern, i) do {} while (0)
#define STAMP_DECL do {} while (0)
template <int I> struct IC { static constexpr int value = I; };
template <int B, int E, class F> DI void static_for(F&& f) {
    if constexpr (B < E) { f(IC<B>{}); static_for<B + 1, E>(f); }
}
DI float bflo(u32 w) { return __builtin_bit_cast(float, w << 16); }
DI float bfhi(u32 w) { return __builtin_bit_cast(float, w & 0xffff0000u); }
DI float bf1(u16 h) { return __builtin_bit_cast(float, (u32)h << 16); }
DI u32 pkbf(float a, float b) { f32x2 v = {a, b}; bf16x2 r = __builtin_convertvector(v, bf16x2); return __builtin_bit_cast(u32, r); }
DI u32 pkh(float a, float b) { f32x2 v = {a, b}; f16x2 r = __builtin_convertvector(v, f16x2); return __builtin_bit_cast(u32, r); }
DI float rbf(float a) { return bflo(pkbf(a, a)); }
template <int CTRL> DI float dppf(float v) {
    return __builtin_bit_cast(float, __builtin_amdgcn_update_dpp(0, __builtin_bit_cast(int, v), CTRL, 0xF, 0xF, true));
}
DI float red8(float v) { v += dppf<0xB1>(v); v += dppf<0x4E>(v); v += dppf<0x141>(v); return v; }
DI float addx32(float v) {
    u32 b = __builtin_bit_cast(u32, v);
    auto r = __builtin_amdgcn_permlane32_swap(b, b, false, false);
    return __builtin_bit_cast(float, (u32)r[0]) + __builtin_bit_cast(float, (u32)r[1]);
}
DI f32x16 mfma32_bf(u32x4 a, u32x4 b, f32x16 c) {
    return __builtin_amdgcn_mfma_f32_32x32x16_bf16(__builtin_bit_cast(bf16x8, a), __builtin_bit_cast(bf16x8, b), c, 0, 0, 0);
}
DI f32x16 mfma32_h(u32x4 a, u32x4 b, f32x16 c) {
    return __builtin_amdgcn_mfma_f32_32x32x16_f16(__builtin_bit_cast(f16x8, a), __builtin_bit_cast(f16x8, b), c, 0, 0, 0);
}
DI f32x4 mfma16_h(u32x4 a, u32x4 b, f32x4 c) {
    return __builtin_amdgcn_mfma_f32_16x16x32_f16(__builtin_bit_cast(f16x8, a), __builtin_bit_cast(f16x8, b), c, 0, 0, 0);
}
DI u32x2 tr_read(const char* p) {
    s16x4 v = __builtin_amdgcn_ds_read_tr16_b64_v4i16((LAS s16x4*)p);
    return __builtin_bit_cast(u32x2, v);
}
DI u32x4 lds16(const char* p) { return *(const u32x4*)p; }
DI void glds16(const void* sbase, u32 voff, u32 lds_addr) {
    asm volatile("s_mov_b32 m0, %2\n\ts_nop 0\n\tglobal_load_lds_dwordx4 %0, %1" ::"v"(voff), "s"(sbase), "s"(lds_addr) : "memory");
}
template <int N> DI void wait_vm() { asm volatile("s_waitcnt vmcnt(%0)" ::"n"(N) : "memory"); }
DI void gload4x(u32x4& a, u32x4& b, u32x4& c, u32x4& d, u32 voff, const char* base) {
    asm volatile("global_load_dwordx4 %0, %4, %5\n\t"
                 "global_load_dwordx4 %1, %4, %5 offset:1024\n\t"
                 "global_load_dwordx4 %2, %4, %5 offset:2048\n\t"
                 "global_load_dwordx4 %3, %4, %5 offset:3072"
                 : "=&v"(a), "=&v"(b), "=&v"(c), "=&v"(d)
                 : "v"(voff), "s"(base)
                 : "memory");
}
DI void gload1(u32x4& a, u32 voff, const char* base) {
    asm volatile("global_load_dwordx4 %0, %1, %2" : "=&v"(a) : "v"(voff), "s"(base) : "memory");
}
DI void gload_u16x2(u32& a, u32& b, u32 voff, const char* base) {
    asm volatile("global_load_ushort %0, %2, %3\n\tglobal_load_ushort %1, %2, %3 offset:64" : "=&v"(a), "=&v"(b) : "v"(voff), "s"(base) : "memory");
}
DI void gload_x4(u32x2 (&v)[4], u32 voff, const char* base) {
    asm volatile("global_load_dwordx2 %0, %4, %5\n\tglobal_load_dwordx2 %1, %4, %5 offset:16\n\t"
                 "global_load_dwordx2 %2, %4, %5 offset:32\n\tglobal_load_dwordx2 %3, %4, %5 offset:48"
                 : "=&v"(v[0]), "=&v"(v[1]), "=&v"(v[2]), "=&v"(v[3]) : "v"(voff), "s"(base) : "memory");
}
DI void gload_s16(u32 (&v)[16], u32 voff, const char* base) {
#define S16L(n, off) "global_load_ushort %" #n ", %16, %17 offset:" #off " sc1\n\t"
    asm volatile(S16L(0, 0) S16L(1, 128) S16L(2, 256) S16L(3, 384) S16L(4, 1024) S16L(5, 1152) S16L(6, 1280) S16L(7, 1408)
                 S16L(8, 2048) S16L(9, 2176) S16L(10, 2304) S16L(11, 2432) S16L(12, 3072) S16L(13, 3200) S16L(14, 3328) S16L(15, 3456) "s_nop 0"
                 : "=&v"(v[0]), "=&v"(v[1]), "=&v"(v[2]), "=&v"(v[3]), "=&v"(v[4]), "=&v"(v[5]), "=&v"(v[6]), "=&v"(v[7]), "=&v"(v[8]),
                   "=&v"(v[9]), "=&v"(v[10]), "=&v"(v[11]), "=&v"(v[12]), "=&v"(v[13]), "=&v"(v[14]), "=&v"(v[15])
                 : "v"(voff), "s"(base) : "memory");
#undef S16L
}
DI void gload_flag(u32& v, u32 voff, const char* base) {
    asm volatile("global_load_dword %0, %1, %2 sc1" : "=&v"(v) : "v"(voff), "s"(base) : "memory");
}
template <int N> DI void wait_vm_tie1(u32& a) { asm volatile("s_waitcnt vmcnt(%1)" : "+v"(a) : "n"(N) : "memory"); }
template <int N> DI void wait_vm_tie2(u32x4& a, u32x4& b) {
    asm volatile("s_waitcnt vmcnt(%2)" : "+v"(a), "+v"(b) : "n"(N) : "memory");
}
template <int N> DI void wait_vm_tie4(u32x4& a, u32x4& b, u32x4& c, u32x4& d) {
    asm volatile("s_waitcnt vmcnt(%4)" : "+v"(a), "+v"(b), "+v"(c), "+v"(d) : "n"(N) : "memory");
}
#define GST_MODE 0
DI void gst16(void* p, u32x4 v) {
    *(u32x4*)p = v;
}
DI void gst16_dev(void* p, u32x4 v) {
    asm volatile("global_store_dwordx4 %0, %1, off sc1\n\ts_nop 1" ::"v"(p), "v"(v) : "memory");
}
DI void gload1_dev(u32x4& a, u32 voff, const char* base) {
    asm volatile("global_load_dwordx4 %0, %1, %2 sc1" : "=&v"(a) : "v"(voff), "s"(base) : "memory");
}
DI void wait_lgkm0() { asm volatile("s_waitcnt lgkmcnt(0)" ::: "memory"); }
DI void bar() { asm volatile("" ::: "memory"); __builtin_amdgcn_s_barrier(); asm volatile("" ::: "memory"); }
DI void lbar() { wait_lgkm0(); bar(); }
DI float dot2bf(u32 a, u32 b, float c) {
    return __builtin_amdgcn_fdot2_f32_bf16(__builtin_bit_cast(bf16x2, a), __builtin_bit_cast(bf16x2, b), c, false);
}
DI void sched_fence() { __builtin_amdgcn_sched_barrier(0); }
DI float gelu_tanh(float x) {
    const float c0 = -2.0f * 0.7978845608028654f * 1.4426950408889634f;
    const float c1 = c0 * 0.044715f;
    float t = __builtin_fmaf(x * x, c1, c0);
    float e = __builtin_amdgcn_exp2f(x * t);
    return x * __builtin_amdgcn_rcpf(1.0f + e);
}

DI float smul(float a, float b) { float r; asm("v_mul_f32 %0, %1, %2" : "=v"(r) : "v"(a), "v"(b)); return r; }
DI float sfma(float a, float b, float c) { float r; asm("v_fma_f32 %0, %1, %2, %3" : "=v"(r) : "v"(a), "v"(b), "v"(c)); return r; }
DI float sfnma(float a, float b, float c) { float r; asm("v_fma_f32 %0, -%1, %2, %3" : "=v"(r) : "v"(a), "v"(b), "v"(c)); return r; }
DI void gelu4(float& x0, float& x1, float& x2, float& x3) {
    const float c1 = -2.0f * 0.7978845608028654f * 1.4426950408889634f * 0.044715f;
    float t0, t1, t2, t3;
    asm("v_mul_f32 %4, %0, %0\n\tv_mul_f32 %5, %1, %1\n\tv_mul_f32 %6, %2, %2\n\tv_mul_f32 %7, %3, %3\n\t"
        "v_fmaak_f32 %4, %4, %8, 0xc0135761\n\tv_fmaak_f32 %5, %5, %8, 0xc0135761\n\tv_fmaak_f32 %6, %6, %8, 0xc0135761\n\tv_fmaak_f32 %7, %7, %8, 0xc0135761\n\t"
        "v_mul_f32 %4, %4, %0\n\tv_mul_f32 %5, %5, %1\n\tv_mul_f32 %6, %6, %2\n\tv_mul_f32 %7, %7, %3\n\t"
        "v_exp_f32 %4, %4\n\tv_exp_f32 %5, %5\n\tv_exp_f32 %6, %6\n\tv_exp_f32 %7, %7\n\t"
        "v_add_f32 %4, 1.0, %4\n\tv_add_f32 %5, 1.0, %5\n\tv_add_f32 %6, 1.0, %6\n\tv_add_f32 %7, 1.0, %7\n\t"
        "v_rcp_f32 %4, %4\n\tv_rcp_f32 %5, %5\n\tv_rcp_f32 %6, %6\n\tv_rcp_f32 %7, %7\n\t"
        "v_mul_f32 %0, %0, %4\n\tv_mul_f32 %1, %1, %5\n\tv_mul_f32 %2, %2, %6\n\tv_mul_f32 %3, %3, %7"
        : "+v"(x0), "+v"(x1), "+v"(x2), "+v"(x3), "=&v"(t0), "=&v"(t1), "=&v"(t2), "=&v"(t3)
        : "v"(c1));
}
DI f32x2 gelu2(f32x2 x) {
    const float c0 = -2.0f * 0.7978845608028654f * 1.4426950408889634f;
    const float c1 = c0 * 0.044715f;
    const f32x2 t = __builtin_elementwise_fma(x * x, (f32x2){c1, c1}, (f32x2){c0, c0}) * x;
    const f32x2 d = (f32x2){__builtin_amdgcn_exp2f(t[0]), __builtin_amdgcn_exp2f(t[1])} + 1.0f;
    return x * (f32x2){__builtin_amdgcn_rcpf(d[0]), __builtin_amdgcn_rcpf(d[1])};
}

constexpr int CV_STG = 32 * 272;
struct cpx { float re, im; };
template <int OFF0, int OFF1> DI void lds_put2(u32 addr, u32 a, u32 b) {
    asm volatile("ds_write2_b32 %0, %1, %2 offset0:%3 offset1:%4" ::"v"(addr), "v"(a), "v"(b), "n"(OFF0), "n"(OFF1) : "memory");
}
DI void cmul4(cpx (&o)[4], const cpx& a0, const cpx& b0, const cpx& a1, const cpx& b1, const cpx& a2, const cpx& b2, const cpx& a3,
              const cpx& b3) {
    asm("v_mul_f32 %0, %8, %10\n\tv_mul_f32 %2, %12, %14\n\tv_mul_f32 %4, %16, %18\n\tv_mul_f32 %6, %20, %22\n\t"
        "v_mul_f32 %1, %8, %11\n\tv_mul_f32 %3, %12, %15\n\tv_mul_f32 %5, %16, %19\n\tv_mul_f32 %7, %20, %23\n\t"
        "v_fma_f32 %0, -%9, %11, %0\n\tv_fma_f32 %2, -%13, %15, %2\n\tv_fma_f32 %4, -%17, %19, %4\n\tv_fma_f32 %6, -%21, %23, %6\n\t"
        "v_fma_f32 %1, %9, %10, %1\n\tv_fma_f32 %3, %13, %14, %3\n\tv_fma_f32 %5, %17, %18, %5\n\tv_fma_f32 %7, %21, %22, %7"
        : "=&v"(o[0].re), "=&v"(o[0].im), "=&v"(o[1].re), "=&v"(o[1].im), "=&v"(o[2].re), "=&v"(o[2].im), "=&v"(o[3].re), "=&v"(o[3].im)
        : "v"(a0.re), "v"(a0.im), "v"(b0.re), "v"(b0.im), "v"(a1.re), "v"(a1.im), "v"(b1.re), "v"(b1.im), "v"(a2.re), "v"(a2.im),
          "v"(b2.re), "v"(b2.im), "v"(a3.re), "v"(a3.im), "v"(b3.re), "v"(b3.im));
}
DI float exp_acc(float x) {
    const float t = x * 1.44269502f;
    const float r = __builtin_fmaf(x, 1.92596299e-8f, __builtin_fmaf(x, 1.44269502f, -t));
    const float e = __builtin_amdgcn_exp2f(t);
    return __builtin_fmaf(e, r * 0.693147182f, e);
}
DI float wave_sum(float v) {
    v = red8(v);
    v += dppf<0x128>(v);
    const int b = __builtin_bit_cast(int, v);
    return (__builtin_bit_cast(float, __builtin_amdgcn_readlane(b, 0)) + __builtin_bit_cast(float, __builtin_amdgcn_readlane(b, 16))) +
           (__builtin_bit_cast(float, __builtin_amdgcn_readlane(b, 32)) + __builtin_bit_cast(float, __builtin_amdgcn_readlane(b, 48)));
}
DI float kgen_wave(char* stg, int lane, float lar, float aim, float cr, float ci, float ldt, float cabs) {
    const float dt = rbf(exp_acc(ldt));
    const float are = -rbf(exp_acc(lar)) * dt, aimd = aim * dt;
    const float bound = wave_sum(cabs) * (2.0f * dt);
    int ex = (int)((__builtin_bit_cast(u32, bound) >> 23) & 0xff);
    ex = ex < 30 ? 30 : (ex > 230 ? 230 : ex);
    const float scl = __builtin_bit_cast(float, (u32)(127 + 14 + 126 - ex) << 23);
    const float a2 = are * 1.4426950408889634f;
    const float thi = aimd * 0.15915494309189535f;
    const float tlo = __builtin_fmaf(aimd, 0.15915494309189535f, -thi) + aimd * 6.4206383e-09f;
    auto cexp = [&](float pw) LAM {
        const float e = __builtin_amdgcn_exp2f(a2 * pw);
        const float th = __builtin_amdgcn_fractf(thi * pw) + tlo * pw;
        return cpx{e * __builtin_amdgcn_cosf(th), e * __builtin_amdgcn_sinf(th)};
    };
    const cpx E1 = cexp(1.0f), E8 = cexp(8.0f), E256 = cexp(256.0f);
    const u32 sa = (u32)(size_t)stg + lane * 4;
    auto pk = [&](const cpx& v) LAM { return pkh(v.re, v.im); };
    const cpx e1 = {E1.re, -E1.im}, g1 = {E256.re, -E256.im};
    const float cs2 = 2.0f * dt * scl;
    const cpx C = {cr * cs2, -ci * cs2};
    cpx t[4], y[4], u[4];
    cmul4(t, E8, E8, e1, e1, g1, g1, C, g1);
    const cpx E16 = t[0], e2 = t[1], g2 = t[2], c256 = t[3];
    cmul4(t, E16, E8, E16, E16, e2, e1, e2, e2);
    const cpx E32 = t[1], e3 = t[2], e4 = t[3];
    y[0] = cpx{1.0f, 0.0f}; y[1] = E8; y[2] = E16; y[3] = t[0];
    cmul4(t, g2, g1, g2, g2, C, g2, C, e1);
    const cpx g3 = t[0], g4 = t[1], c512 = t[2], r1 = t[3];
#pragma unroll
    for (int jj = 0; jj < 8; ++jj) {
        lds_put2<0, 68>(sa + jj * 1088, pk(y[0]), pk(y[1]));
        lds_put2<136, 204>(sa + jj * 1088, pk(y[2]), pk(y[3]));
        if (jj < 7) { cmul4(u, y[0], E32, y[1], E32, y[2], E32, y[3], E32); y[0] = u[0]; y[1] = u[1]; y[2] = u[2]; y[3] = u[3]; }
    }
    const int r = lane & 31, h = lane >> 5;
    u32x4 bfr[8];
#pragma unroll
    for (int kk = 0; kk < 8; ++kk) bfr[kk] = lds16(stg + r * 272 + (8 * kk + 4 * h) * 4);
    cpx e567[4], g5678[4];
    cmul4(e567, e4, e1, e4, e2, e4, e3, C, g3);
    cmul4(g5678, g4, g1, g4, g2, g4, g3, g4, g4);
    const cpx c768 = e567[3];
    lds_put2<0, 68>(sa, pk(C), pk(r1));
    cmul4(t, C, e2, C, e3, C, e4, C, e567[0]);
    lds_put2<136, 204>(sa, pk(t[0]), pk(t[1]));
    lds_put2<0, 68>(sa + 4 * 272, pk(t[2]), pk(t[3]));
    cmul4(t, C, e567[1], C, e567[2], c256, e2, c256, e4);
    lds_put2<136, 204>(sa + 4 * 272, pk(t[0]), pk(t[1]));
    lds_put2<0, 68>(sa + 8 * 272, pk(c256), pk(t[2]));
    cmul4(u, c256, e567[1], c512, e4, c768, e4, C, g4);
    lds_put2<136, 204>(sa + 8 * 272, pk(t[3]), pk(u[0]));
    lds_put2<0, 68>(sa + 12 * 272, pk(c512), pk(u[1]));
    lds_put2<136, 204>(sa + 12 * 272, pk(c768), pk(u[2]));
    cmul4(t, C, g5678[0], C, g5678[1], C, g5678[2], C, g5678[3]);
    lds_put2<0, 68>(sa + 16 * 272, pk(u[3]), pk(t[0]));
    lds_put2<136, 204>(sa + 16 * 272, pk(t[1]), pk(t[2]));
    *(u32*)(stg + 20 * 272 + lane * 4) = pk(t[3]);
    u32x4 afr[8];
#pragma unroll
    for (int kk = 0; kk < 8; ++kk) afr[kk] = lds16(stg + r * 272 + (8 * kk + 4 * h) * 4);
    sched_fence();
    f32x16 acc;
#pragma unroll
    for (int i = 0; i < 16; ++i) acc[i] = 0.f;
#pragma unroll
    for (int kk = 0; kk < 8; ++kk) acc = mfma32_h(afr[kk], bfr[kk], acc);
#pragma unroll
    for (int g = 0; g < 3; ++g)
        *(f32x4*)(stg + (g * 64 + lane) * 16) = (f32x4){acc[4 * g], acc[4 * g + 1], acc[4 * g + 2], acc[4 * g + 3]};
    return __builtin_bit_cast(float, (u32)(127 - 14 - 126 + ex) << 23);
}
DI void kgen_reduce(const char* pin, char* tout, int tid) {
    if (tid < 192) {
        const char* pp = pin + tid * 16;
        const f32x4 a = *(const f32x4*)pp, b = *(const f32x4*)(pp + CV_STG), c = *(const f32x4*)(pp + 2 * CV_STG), d = *(const f32x4*)(pp + 3 * CV_STG);
        *(f32x4*)(tout + tid * 16) = (a + b) + (c + d);
    }
}
DI float ktab(const char* tin, int idx) {
    const int row = idx >> 5, col = idx & 31;
    return *(const float*)(tin + (((row >> 3) * 64 + col + 32 * ((row >> 2) & 1)) * 4 + (row & 3)) * 4);
}
DI u32x4 kgen_expand(const char* tin, int z) {
    float kv[8];
    if (z < 32) {
#pragma unroll
        for (int e = 0; e < 8; ++e) kv[e] = ktab(tin, e * 32 + z);
    } else if (z < 64) {
        const int r = z - 32;
        const float v0 = ktab(tin, 256 + r), v2 = ktab(tin, 288 + r), v4 = ktab(tin, 320 + r), v6 = ktab(tin, 352 + r), v8 = ktab(tin, z == 63 ? 384 : 257 + r);
        kv[0] = v0; kv[1] = v0; kv[2] = v2; kv[3] = v4; kv[4] = v4; kv[5] = v4; kv[6] = v6; kv[7] = v8;
    } else if (z < 128) {
        const int r = z - 64, r1 = r + 1;
        const int i0 = (12 + 2 * (r >> 5)) * 32 + (r & 31), i8 = (12 + 2 * (r1 >> 5)) * 32 + (r1 & 31);
        const float v0 = ktab(tin, i0), v4 = ktab(tin, i0 + 32), v8 = ktab(tin, i8);
        kv[0] = v0; kv[1] = v0; kv[2] = v0; kv[3] = v4; kv[4] = v4; kv[5] = v4; kv[6] = v8; kv[7] = v8;
    } else {
        const float v0 = ktab(tin, z + 384), v8 = ktab(tin, z + 385);
        kv[0] = v0; kv[1] = v0; kv[2] = v0; kv[3] = v0; kv[4] = (z & 1) ? v8 : v0; kv[5] = v8; kv[6] = v8; kv[7] = v8;
    }
    return (u32x4){pkh(kv[7], kv[6]), pkh(kv[5], kv[4]), pkh(kv[3], kv[2]), pkh(kv[1], kv[0])};
}
struct KRaw { u32 v[11]; };
DI void kgen_load(KRaw& p, int d, int n, const u16* logA, const u16* Aim, const u16* Cre, const u16* Cim, const u16* logdt) {
    const u32 vo = (u32)((d * NST + n) * 2), v1 = (u32)((d * NST + (n ^ 64)) * 2), v2 = (u32)((d * NST + (n ^ 128)) * 2),
              v3 = (u32)((d * NST + (n ^ 192)) * 2), vd = (u32)(d * 2);
    asm volatile("global_load_ushort %0, %11, %16\n\tglobal_load_ushort %1, %11, %17\n\tglobal_load_ushort %2, %11, %18\n\t"
                 "global_load_ushort %3, %11, %19\n\tglobal_load_ushort %4, %15, %20\n\t"
                 "global_load_ushort %5, %12, %18\n\tglobal_load_ushort %6, %12, %19\n\tglobal_load_ushort %7, %13, %18\n\t"
                 "global_load_ushort %8, %13, %19\n\tglobal_load_ushort %9, %14, %18\n\tglobal_load_ushort %10, %14, %19"
                 : "=&v"(p.v[0]), "=&v"(p.v[1]), "=&v"(p.v[2]), "=&v"(p.v[3]), "=&v"(p.v[4]), "=&v"(p.v[5]), "=&v"(p.v[6]), "=&v"(p.v[7]),
                   "=&v"(p.v[8]), "=&v"(p.v[9]), "=&v"(p.v[10])
                 : "v"(vo), "v"(v1), "v"(v2), "v"(v3), "v"(vd), "s"(logA), "s"(Aim), "s"(Cre), "s"(Cim), "s"(logdt)
                 : "memory");
}
template <int N> DI void kgen_wait(KRaw& p) {
    asm volatile("s_waitcnt vmcnt(%11)"
                 : "+v"(p.v[0]), "+v"(p.v[1]), "+v"(p.v[2]), "+v"(p.v[3]), "+v"(p.v[4]), "+v"(p.v[5]), "+v"(p.v[6]), "+v"(p.v[7]), "+v"(p.v[8]),
                   "+v"(p.v[9]), "+v"(p.v[10])
                 : "n"(N) : "memory");
}
DI float kgen_wave_raw(char* stg, int lane, const KRaw& p) {
    float cabs = 0.f;
#pragma unroll
    for (int k = 0; k < 4; ++k) {
        const int ia = k == 0 ? 2 : 3 + 2 * k, ib = k == 0 ? 3 : 4 + 2 * k;
        cabs += __builtin_fabsf(bf1((u16)p.v[ia])) + __builtin_fabsf(bf1((u16)p.v[ib]));
    }
    return kgen_wave(stg, lane, bf1((u16)p.v[0]), bf1((u16)p.v[1]), bf1((u16)p.v[2]), bf1((u16)p.v[3]), bf1((u16)p.v[4]), cabs);
}
DI void soft_bar(char* cnt, int lane, u32 target) {
    wait_lgkm0();
    if (lane == 0) __hip_atomic_fetch_add((LAS u32*)cnt, 1u, __ATOMIC_RELAXED, __HIP_MEMORY_SCOPE_WORKGROUP);
    for (int spin = 0; spin < (1 << 22); ++spin) {
        if (__hip_atomic_load((LAS u32*)cnt, __ATOMIC_RELAXED, __HIP_MEMORY_SCOPE_WORKGROUP) >= target) break;
        asm volatile("s_sleep 1" ::: "memory");
    }
    asm volatile("" ::: "memory");
}

DI void soft_arrive(char* cnt, int lane) {
    wait_lgkm0();
    if (lane == 0) __hip_atomic_fetch_add((LAS u32*)cnt, 1u, __ATOMIC_RELAXED, __HIP_MEMORY_SCOPE_WORKGROUP);
}
DI void soft_wait(char* cnt, u32 target) {
    for (int spin = 0; spin < (1 << 22); ++spin) {
        if (__hip_atomic_load((LAS u32*)cnt, __ATOMIC_RELAXED, __HIP_MEMORY_SCOPE_WORKGROUP) >= target) break;
        asm volatile("s_sleep 1" ::: "memory");
    }
    asm volatile("" ::: "memory");
}

DI void tile_map(int b, int& panel, int& grp) {
    const int xcd = b & 7, idx = b >> 3, y = xcd >> 1, side = xcd & 1;
    if (idx < 24) { panel = 8 * y + (idx & 7); grp = (idx >> 3) + 4 * side; }
    else { panel = 8 * y + 4 * side + (idx - 24); grp = 3; }
}
DI void l2_touch(u32& sink, const char* base, int lane) {
    asm volatile("global_load_dword %0, %1, %2" : "+v"(sink) : "v"(lane * 64), "s"(base) : "memory");
}

constexpr int K1_XSTRIDE = 912;
#define K1R 7
constexpr int K1_RING = K1R;
constexpr int K1_EARLY = 2;
constexpr int K1_OFF_RING = 64 * K1_XSTRIDE;
constexpr int K1_OFF_PS = K1_OFF_RING + 4 * K1_RING * 2048;
constexpr int K1_OFF_ST = K1_OFF_PS + 2048;
constexpr int K1_OFF_CNT = K1_OFF_ST + 256;
constexpr int KG_BYTES = 4 * CV_STG + 2 * 3072;
constexpr int K1_OFF_KG = K1_OFF_CNT + 32;
constexpr int K1_LDS = K1_OFF_KG + KG_BYTES;
static_assert(K1_LDS <= 163840 && 2 * KG_BYTES + 64 <= K1_LDS, "LDS");
DI bool kgen_in_k2(int d) { return d < 32 || (d >= 256 && d < 288); }
DI int kgen_k1_channel(int q) { return q < 224 ? 32 + q : 64 + q; }
DI void kgen_channel(char* kgb, char* cnt, int round, const KRaw& pr, int ft, int lane, int d, char* ws) {
    const int n = ft * 64 + lane;
    char* T = kgb + 4 * CV_STG + (round & 1) * 3072;
    const float kinv = kgen_wave_raw(kgb + ft * CV_STG, lane, pr);
    soft_bar(cnt, lane, 4 * (2 * round + 1));
    kgen_reduce(kgb, T, n);
    soft_bar(cnt, lane, 4 * (2 * round + 2));
    gst16(ws + OFF_KR + (size_t)d * 4096 + n * 16, kgen_expand(T, n));
    if (n == 0) *(float*)(ws + OFF_KI + d * 4) = kinv;
}
constexpr u32 TAG_C = 0x9E3779B9u, TAG_X = 0x5BD1E995u;

__global__ __launch_bounds__(512, 2) void k1_kernel(const u16* x, const u16* w_in, const u16* w_out, const u16* w_sp, const u16* logA,
                                                    const u16* Aim, const u16* Cre, const u16* Cim, const u16* logdt, char* ws) {
    extern __shared__ __attribute__((aligned(16))) char smem[];
    const int tid = threadIdx.x, lane = tid & 63;
    const int wid = __builtin_amdgcn_readfirstlane(tid >> 6);
    const int b = blockIdx.x;
    if (b >= 224) {
        const int a = b - 224, grp = wid >> 2, ftk = wid & 3, nch = 3 - grp, q0 = 224 + 5 * a + 3 * grp;
        char* cnt = smem + grp * 16;
        char* kgb = smem + 64 + grp * KG_BYTES;
        STAMP_DECL;
        STAMP(0, 0);
        KRaw pr[3];
#pragma unroll
        for (int c = 0; c < 3; ++c) kgen_load(pr[c], kgen_k1_channel(q0 + (c < nch ? c : 0)), ftk * 64 + lane, logA, Aim, Cre, Cim, logdt);
        if (tid < 8) *(u32*)(smem + tid * 4) = 0;
        if (a == 0) for (int i = tid; i < FL_WORDS; i += 512) *(u32*)(ws + OFF_FL + i * 4) = 0;
        lbar();
        if (grp == 1) {
            const int u = a * 256 + (tid - 256);
            if (u < 28 * 2 * 2 * 56) {
                const int o = u % 56, r = u / 56, h = r & 1, s = (r >> 1) & 1, ft = r >> 2;
                u32x4 rows[8];
#pragma unroll
                for (int j = 0; j < 8; ++j) {
                    const int f = 32 * ft + 16 * s + 8 * (j >> 2) + 4 * h + (j & 3);
                    rows[j] = *(const u32x4*)(w_out + (size_t)f * DM + 8 * o);
                }
                char* wo = ws + OFF_WO;
#pragma unroll
                for (int dd = 0; dd < 8; ++dd) {
                    u32x4 v;
#pragma unroll
                    for (int k = 0; k < 4; ++k) {
                        const u32 ra = rows[2 * k][dd >> 1], rb = rows[2 * k + 1][dd >> 1];
                        v[k] = (dd & 1) ? ((ra >> 16) | (rb & 0xffff0000u)) : ((ra & 0xffffu) | (rb << 16));
                    }
                    const int d = 8 * o + dd, dt = d >> 5, dl = d & 31;
                    gst16(wo + ((size_t)(((dt * 28 + ft) * 2 + s) * 64 + dl + 32 * h)) * 16, v);
                }
            }
        }
#pragma unroll
        for (int c = 0; c < 3; ++c) kgen_wait<0>(pr[c]);
        STAMP(0, 1);
#pragma unroll
        for (int c = 0; c < 3; ++c) {
            if (c < nch) kgen_channel(kgb, cnt, c, pr[c], ftk, lane, kgen_k1_channel(q0 + c), ws);
            if (c == 0) STAMP(0, 2);
            if (c == 1) STAMP(0, 3);
            if (c == 2) STAMP(0, 4);
        }
        STAMP(0, 14);
        return;
    }
    int p, j;
    tile_map(b, p, j);
    const u32 lds0 = (u32)(size_t)smem;
    STAMP_DECL;
    STAMP(0, 0);
    const int ft = wid & 3;
    auto load_x = [&]() LAM {
        if (lane < 56) {
            const char* xb = (const char*)x + (size_t)(64 * p + 8 * wid) * (DM * 2);
#pragma unroll
            for (int i = 0; i < 8; ++i) glds16(xb + i * (DM * 2), lane * 16, lds0 + (8 * wid + i) * K1_XSTRIDE);
        }
    };
    auto center_x = [&]() LAM {
        const int row = 8 * wid + (lane >> 3), t8 = lane & 7;
        char* xp = smem + row * K1_XSTRIDE + t8 * 16;
        float s0 = 0.f, s1 = 0.f, q0 = 0.f, q1 = 0.f;
        u32x4 xr[7];
#pragma unroll
        for (int i = 0; i < 7; ++i) xr[i] = lds16(xp + i * 128);
        sched_fence();
#pragma unroll
        for (int i = 0; i < 7; ++i) {
            const u32x4 v = xr[i];
            s0 = dot2bf(v[0], 0x3F803F80u, s0); s1 = dot2bf(v[1], 0x3F803F80u, s1);
            q0 = dot2bf(v[0], v[0], q0); q1 = dot2bf(v[1], v[1], q1);
            s0 = dot2bf(v[2], 0x3F803F80u, s0); s1 = dot2bf(v[3], 0x3F803F80u, s1);
            q0 = dot2bf(v[2], v[2], q0); q1 = dot2bf(v[3], v[3], q1);
        }
        const float mean = red8(s0 + s1) * (1.0f / DM);
        const float var = __builtin_fmaxf(red8(q0 + q1) * (1.0f / DM) - mean * mean, 0.f);
        const float mb = rbf(mean);
#pragma unroll
        for (int i = 0; i < 7; ++i) {
            u32x4 o;
#pragma unroll
            for (int k = 0; k < 4; ++k) o[k] = pkbf(bflo(xr[i][k]) - mb, bfhi(xr[i][k]) - mb);
            *(u32x4*)(xp + i * 128) = o;
        }
        const float sd = rbf(__builtin_amdgcn_sqrtf(rbf(rbf(var) + 1e-5f)));
        if (t8 == 0) *(float*)(smem + K1_OFF_ST + row * 4) = __builtin_amdgcn_rcpf(sd);
    };

    if (wid >= 4) {
        load_x();
        KRaw pr;
        kgen_load(pr, kgen_k1_channel(b), ft * 64 + lane, logA, Aim, Cre, Cim, logdt);
        bar();
        __builtin_amdgcn_s_setprio(2);
        wait_vm<11>();
        center_x();
        soft_arrive(smem + K1_OFF_CNT + 4, lane);
        kgen_wait<0>(pr);
        __builtin_amdgcn_s_setprio(0);
        kgen_channel(smem + K1_OFF_KG, smem + K1_OFF_CNT + 8, 0, pr, ft, lane, kgen_k1_channel(b), ws);
        {
            u32 sink = 0;
#pragma unroll
            for (int r = 0; r < 3; ++r) {
                int row = (b * 4 + ft) * 3 + r;
                row = row < SL ? row : SL - 1;
                l2_touch(sink, (const char*)w_sp + ((size_t)row << 12), lane);
            }
            asm volatile("s_waitcnt vmcnt(0)" : "+v"(sink)::"memory");
        }
        STAMP(0, 14);
        return;
    }
    u32 epoch;
    {
        const char* ep = ws + OFF_EP + p * 128;
        const u32 zero = 0;
        asm volatile("global_load_dword %0, %1, %2 sc1" : "=&v"(epoch) : "v"(zero), "s"(ep) : "memory");
    }
    load_x();
    const u32 wvoff = (u32)(((lane >> 2) * (2 * FH) + 128 * j + 32 * ft + (lane & 3) * 8) * 2);
    const u32 ring = lds0 + K1_OFF_RING + ft * (K1_RING * 2048);
    auto issue_w1 = [&](auto I, int z1) LAM {
        constexpr int ks = I.value, slot = ks % K1_RING;
        glds16((const char*)w_in + (size_t)ks * 16 * (2 * FH) * 2 + (z1 ? 0 : FH * 2), wvoff, ring + slot * 2048 + z1 * 1024);
    };
    auto issue_w = [&](auto I) LAM { issue_w1(I, 0); issue_w1(I, 1); };
    static_for<0, K1_EARLY>(issue_w);
    if (tid < 8) *(u32*)(smem + K1_OFF_CNT + tid * 4) = 0;
    lbar();
    __builtin_amdgcn_s_setprio(2);
    asm volatile("s_waitcnt vmcnt(%1)" : "+v"(epoch) : "n"(2 * K1_EARLY) : "memory");
    STAMP(0, 1);
    center_x();
    soft_arrive(smem + K1_OFF_CNT + 4, lane);
    STAMP(0, 2);
    static_for<K1_EARLY, K1_RING>(issue_w);
    soft_wait(smem + K1_OFF_CNT + 4, 8);
    __builtin_amdgcn_s_setprio(3);
    STAMP(0, 3);

    f32x16 a2[2], a1[2];
#pragma unroll
    for (int i = 0; i < 16; ++i) { a2[0][i] = 0.f; a2[1][i] = 0.f; a1[0][i] = 0.f; a1[1][i] = 0.f; }
    {
        const int h = lane >> 5, blk = (lane >> 4) & 1, q = (lane & 15) >> 2, pp = lane & 3;
        const char* aptr = smem + K1_OFF_RING + ft * (K1_RING * 2048) + (8 * h + q) * 64 + 32 * blk + 8 * pp;
        const char* bptr = smem + (lane & 31) * K1_XSTRIDE + h * 16;
        u32x4 f2[3], f1[3], b0[3], b1[3];
        auto rd_a = [&](auto I) LAM {
            constexpr int ks = I.value, cur = ks % 3, so = (ks % K1_RING) * 2048;
            const u32x2 l2 = tr_read(aptr + so), h2 = tr_read(aptr + so + 256);
            f2[cur] = (u32x4){l2[0], l2[1], h2[0], h2[1]};
            b0[cur] = lds16(bptr + ks * 32);
        };
        auto rd_b = [&](auto I) LAM {
            constexpr int ks = I.value, cur = ks % 3, so = (ks % K1_RING) * 2048;
            b1[cur] = lds16(bptr + 32 * K1_XSTRIDE + ks * 32);
            const u32x2 l1 = tr_read(aptr + so + 1024), h1 = tr_read(aptr + so + 1280);
            f1[cur] = (u32x4){l1[0], l1[1], h1[0], h1[1]};
        };
        wait_vm<2 * (K1_RING - 1)>();
        rd_a(IC<0>{}); rd_b(IC<0>{});
        wait_vm<2 * (K1_RING - 2)>();
        rd_a(IC<1>{}); rd_b(IC<1>{});
        static_for<0, 28>([&](auto I) LAM {
            constexpr int ks = I.value, cur = ks % 3;
            sched_fence();
            a2[0] = mfma32_bf(f2[cur], b0[cur], a2[0]);
            sched_fence();
            if constexpr (ks + K1_RING < 28) issue_w1(IC<ks + K1_RING>{}, 0);
            sched_fence();
            a2[1] = mfma32_bf(f2[cur], b1[cur], a2[1]);
            sched_fence();
            if constexpr (ks + 2 < 28) {
                constexpr int issued = (ks + K1_RING - 1 < 27) ? ks + K1_RING - 1 : 27;
                constexpr int extra = (ks + K1_RING < 28) ? 1 : 0;
                wait_vm<2 * (issued - (ks + 2)) + extra>();
                rd_a(IC<ks + 2>{});
            }
            sched_fence();
            a1[0] = mfma32_bf(f1[cur], b0[cur], a1[0]);
            sched_fence();
            if constexpr (ks + K1_RING < 28) issue_w1(IC<ks + K1_RING>{}, 1);
            sched_fence();
            a1[1] = mfma32_bf(f1[cur], b1[cur], a1[1]);
            sched_fence();
            if constexpr (ks + 2 < 28) rd_b(IC<ks + 2>{});
        });
    }
    STAMP(0, 4);

    asm volatile("s_nop 7\n\ts_nop 7" : "+v"(a2[0]), "+v"(a2[1]), "+v"(a1[0]), "+v"(a1[1]));
    const int h = lane >> 5;
    float ri[2];
#pragma unroll
    for (int st = 0; st < 2; ++st) ri[st] = *(const float*)(smem + K1_OFF_ST + (32 * st + (lane & 31)) * 4);
    auto fold = [&](f32x16 (&acc)[2], int) LAM {
#pragma unroll
        for (int st = 0; st < 2; ++st)
#pragma unroll
            for (int i = 0; i < 16; ++i) acc[st][i] = smul(acc[st][i], ri[st]);
    };
    const int ftile = 4 * j + ft;
    fold(a2, 0);
    u32 zp[2][8];
    {
        float mw[2], m2[2];
#pragma unroll
        for (int st = 0; st < 2; ++st) {
            float s0 = 0.f, q0 = 0.f;
#pragma unroll
            for (int i = 0; i < 8; i += 2) {
                const u32 c = pkbf(a2[st][2 * i], a2[st][2 * i + 1]), c2 = pkbf(a2[st][2 * i + 2], a2[st][2 * i + 3]);
                float g0 = bflo(c), g1 = bfhi(c), g2 = bflo(c2), g3 = bfhi(c2);
                gelu4(g0, g1, g2, g3);
                zp[st][i] = pkbf(g0, g1); zp[st][i + 1] = pkbf(g2, g3);
                s0 = dot2bf(zp[st][i], 0x3F803F80u, s0); q0 = dot2bf(zp[st][i], zp[st][i], q0);
                s0 = dot2bf(zp[st][i + 1], 0x3F803F80u, s0); q0 = dot2bf(zp[st][i + 1], zp[st][i + 1], q0);
            }
            const float sum = addx32(s0), sq = addx32(q0);
            mw[st] = sum * (1.0f / 32.0f);
            m2[st] = __builtin_fmaxf(sq - sum * mw[st], 0.f);
        }
        f32x2 pv = {h ? mw[1] : mw[0], h ? m2[1] : m2[0]};
        *(f32x2*)(smem + K1_OFF_PS + (ft * 64 + lane) * 8) = pv;
    }
    STAMP(0, 5);
    const u32 tagA = epoch + TAG_C, tagB = tagA ^ TAG_X;
    char* gr = ws + OFF_GR + (size_t)p * 7 * 1024;
    const __amdgpu_buffer_rsrc_t grs = __builtin_amdgcn_make_buffer_rsrc(gr, 0, 7 * 1024, 0x00020000);
    {
        wait_lgkm0();
        u32 old = 0;
        if (lane == 0) old = __hip_atomic_fetch_add((LAS u32*)(smem + K1_OFF_CNT), 1u, __ATOMIC_RELAXED, __HIP_MEMORY_SCOPE_WORKGROUP);
        old = __builtin_amdgcn_readfirstlane(old);
        if (old == 3) {
            f32x2 a[4];
#pragma unroll
            for (int w = 0; w < 4; ++w) a[w] = *(const f32x2*)(smem + K1_OFF_PS + (w * 64 + lane) * 8);
            const float mean = (a[0][0] + a[1][0] + a[2][0] + a[3][0]) * 0.25f;
            float m2 = a[0][1] + a[1][1] + a[2][1] + a[3][1];
#pragma unroll
            for (int w = 0; w < 4; ++w) { const float dlt = a[w][0] - mean; m2 = __builtin_fmaf(32.0f * dlt, dlt, m2); }
            u32x4 g = {__builtin_bit_cast(u32, mean), tagA, __builtin_bit_cast(u32, m2), tagB};
            __builtin_amdgcn_raw_buffer_store_b128(g, grs, j * 1024 + lane * 16, 0, 16);
        }
    }
    __builtin_amdgcn_s_setprio(1);
    STAMP(0, 6);
    u32x4 g[7];
    fold(a1, 1);
    {
        char* z1 = ws + OFF_Z1;
#pragma unroll
        for (int st = 0; st < 2; ++st)
#pragma unroll
            for (int hf = 0; hf < 2; ++hf) {
                if (st == 1 && hf == 1) {
                    sched_fence();
#pragma unroll
                    for (int w = 0; w < 7; ++w) g[w] = __builtin_amdgcn_raw_buffer_load_b128(grs, w * 1024 + lane * 16, 0, 16);
                    sched_fence();
                }
                u32x4 v;
#pragma unroll
                for (int k = 0; k < 4; k += 2) {
                    const u32 c = pkbf(a1[st][8 * hf + 2 * k], a1[st][8 * hf + 2 * k + 1]), c2 = pkbf(a1[st][8 * hf + 2 * k + 2], a1[st][8 * hf + 2 * k + 3]);
                    float g0 = bflo(c), g1 = bfhi(c), g2 = bflo(c2), g3 = bfhi(c2);
                    gelu4(g0, g1, g2, g3);
                    v[k] = pkbf(g0, g1); v[k + 1] = pkbf(g2, g3);
                }
                gst16(z1 + ((size_t)((ftile * 64 + 2 * p + st) * 2 + hf) * 64 + lane) * 16, v);
            }
    }
    {
        u16* xnt = (u16*)(ws + OFF_XNT);
        const int g = lane >> 4, q = (lane & 15) >> 2, pp = lane & 3, i16 = lane & 15;
#pragma unroll
        for (int sh = 0; sh < 2; ++sh) {
            const int dbase = 64 * j + 16 * ft;
            const char* a = smem + (32 * sh + 8 * g + q) * K1_XSTRIDE + (dbase + 4 * pp) * 2;
            const u32x2 lo = tr_read(a), hi = tr_read(a + 4 * K1_XSTRIDE);
            const u32 raw[4] = {lo[0], lo[1], hi[0], hi[1]};
            const f32x4 r0 = *(const f32x4*)(smem + K1_OFF_ST + (32 * sh + 8 * g) * 4), r1 = *(const f32x4*)(smem + K1_OFF_ST + (32 * sh + 8 * g + 4) * 4);
            u32x4 o;
#pragma unroll
            for (int k = 0; k < 4; ++k) {
                const float ra = k < 2 ? r0[2 * k] : r1[2 * k - 4], rb = k < 2 ? r0[2 * k + 1] : r1[2 * k - 3];
                o[k] = pkbf(bflo(raw[k]) * ra, bfhi(raw[k]) * rb);
            }
            gst16(xnt + (size_t)(dbase + i16) * SL + 64 * p + 32 * sh + 8 * g, o);
        }
    }
    STAMP(0, 7);
    __builtin_amdgcn_s_setprio(3);
    float mb, rinv;
    {
        for (int spin = 0; spin < (1 << 22); ++spin) {
            u32 bad = 0;
#pragma unroll
            for (int w = 0; w < 7; ++w) bad |= (g[w][1] ^ tagA) | (g[w][3] ^ tagB);
            if (__all(bad == 0)) break;
            asm volatile("" ::: "memory");
#pragma unroll
            for (int w = 0; w < 7; ++w) g[w] = __builtin_amdgcn_raw_buffer_load_b128(grs, w * 1024 + lane * 16, 0, 16);
        }
        float mean = 0.f, m2 = 0.f;
#pragma unroll
        for (int w = 0; w < 7; ++w) { mean += __builtin_bit_cast(float, (u32)g[w][0]); m2 += __builtin_bit_cast(float, (u32)g[w][2]); }
        mean *= (1.0f / 7.0f);
#pragma unroll
        for (int w = 0; w < 7; ++w) { const float dlt = __builtin_bit_cast(float, (u32)g[w][0]) - mean; m2 = __builtin_fmaf(128.0f * dlt, dlt, m2); }
        mb = rbf(mean);
        const float vb = rbf(m2 * (1.0f / FH));
        rinv = __builtin_amdgcn_rcpf(rbf(__builtin_amdgcn_sqrtf(rbf(vb + 1e-5f))));
        if (j == 0 && ft == 0 && lane == 0)
            __hip_atomic_store((u32*)(ws + OFF_EP + p * 128), tagA, __ATOMIC_RELAXED, __HIP_MEMORY_SCOPE_AGENT);
    }
    STAMP(0, 8);
    {
        char* tb = smem + K1_OFF_RING + ft * (K1_RING * 2048);
        u32 mbb = __builtin_bit_cast(u32, mb), rib = __builtin_bit_cast(u32, rinv);
        auto sm = __builtin_amdgcn_permlane32_swap(mbb, mbb, false, false);
        auto sr = __builtin_amdgcn_permlane32_swap(rib, rib, false, false);
#pragma unroll
        for (int st = 0; st < 2; ++st) {
            const float m = __builtin_bit_cast(float, (u32)sm[st]), r = __builtin_bit_cast(float, (u32)sr[st]);
#pragma unroll
            for (int g = 0; g < 4; ++g) {
                u32x2 v;
#pragma unroll
                for (int k = 0; k < 2; ++k) {
                    const u32 zz = zp[st][2 * g + k];
                    const u32 c = pkbf(bflo(zz) - m, bfhi(zz) - m);
                    v[k] = pkbf(bflo(c) * r, bfhi(c) * r);
                }
                *(u32x2*)(tb + (32 * st + (lane & 31)) * 72 + (8 * g + 4 * h) * 2) = v;
            }
        }
        const int blk = (lane >> 4) & 1, q = (lane & 15) >> 2, pp = lane & 3;
        char* z2t = ws + OFF_Z2T;
#pragma unroll
        for (int ks = 0; ks < 4; ++ks) {
            const char* a = tb + (16 * ks + 8 * h + q) * 72 + 32 * blk + 8 * pp;
            const u32x2 lo = tr_read(a), hi = tr_read(a + 4 * 72);
            gst16(z2t + ((size_t)(ftile * 128 + 4 * p + ks) * 64 + lane) * 16, (u32x4){lo[0], lo[1], hi[0], hi[1]});
        }
    }
    STAMP(0, 14);
}

constexpr int CV_CS = 322 * 16;
constexpr int CV_OFF_KR = 8 * CV_CS;
constexpr int K2_LDS = 6 * 8192;
constexpr int CV_OFF_PB = 0;
constexpr int G2_SLOTS = 6;
constexpr int G2_ZD = 5;
constexpr int g2_ops(int i) { return (i + G2_SLOTS - 1 < 32 ? 2 : 0) + (i + G2_ZD < 32 ? 4 : 0) + (i == 27 ? 6 : 0); }
constexpr int g2_younger(int c) {
    int n = 0;
    const int wi = (c + 1) - (G2_SLOTS - 1);
    const int zi = c - G2_ZD;
    const bool need_w = c + 1 < 32;
    if (need_w && wi >= 0 && wi > zi) {
        n = g2_ops(wi) - 2;
        for (int i = wi + 1; i < c; ++i) n += g2_ops(i);
        return n;
    }
    if (zi >= 0 && (!need_w || zi >= wi)) {
        n = (zi == 27 ? 6 : 0);
        for (int i = zi + 1; i < c; ++i) n += g2_ops(i);
        return n;
    }
    const int lastp = need_w ? 6 * (c + 1) + 1 : 6 * c + 5;
    n = 6 * G2_ZD - 1 - lastp;
    for (int i = 0; i < c; ++i) n += g2_ops(i);
    return n;
}
static_assert(G2_SLOTS - 1 == G2_ZD, "prologue assumes equal depth");

#define KM(c) (c)
DI void silu4(float& x0, float& x1, float& x2, float& x3) {
    const float c = -1.4426950408889634f;
    float t0, t1, t2, t3;
    asm("v_mul_f32 %4, %8, %0\n\tv_mul_f32 %5, %8, %1\n\tv_mul_f32 %6, %8, %2\n\tv_mul_f32 %7, %8, %3\n\t"
        "v_exp_f32 %4, %4\n\tv_exp_f32 %5, %5\n\tv_exp_f32 %6, %6\n\tv_exp_f32 %7, %7\n\t"
        "v_add_f32 %4, 1.0, %4\n\tv_add_f32 %5, 1.0, %5\n\tv_add_f32 %6, 1.0, %6\n\tv_add_f32 %7, 1.0, %7\n\t"
        "v_rcp_f32 %4, %4\n\tv_rcp_f32 %5, %5\n\tv_rcp_f32 %6, %6\n\tv_rcp_f32 %7, %7\n\t"
        "v_mul_f32 %0, %0, %4\n\tv_mul_f32 %1, %1, %5\n\tv_mul_f32 %2, %2, %6\n\tv_mul_f32 %3, %3, %7"
        : "+v"(x0), "+v"(x1), "+v"(x2), "+v"(x3), "=&v"(t0), "=&v"(t1), "=&v"(t2), "=&v"(t3)
        : "v"(c));
}

constexpr int G3_AD = 6;
constexpr int G3_BD = 9;
constexpr int g3_younger(int k) {
    int pos = 0, need = -1;
    for (int q = 0; q < G3_BD; ++q) { pos += 2; if (q == k) need = pos; }
    for (int i = 0; i < k; ++i) {
        if (i + G3_AD < 14) { pos += 2; if (i + G3_AD == k + 1) need = pos > need ? pos : need; }
        if (i + G3_BD < 14) { pos += 2; if (i + G3_BD == k) need = pos > need ? pos : need; }
        if (i == 3) pos += 1;
        if (i == 7) pos += 20;
    }
    return pos - need;
}
DI void gemm2_role(int b, char* smem, const u16* w_sp, const u16* b_sp, const u16* x, u16* out, char* ws) {
    const int tid = threadIdx.x, lane = tid & 63;
    const int wid = __builtin_amdgcn_readfirstlane(tid >> 6);
    int tp, j;
    tile_map(b, tp, j);
    const int ftile = 4 * j + wid;
    const u32 lds0 = (u32)(size_t)smem;
    STAMP_DECL;
    STAMP(1, 0);
    u32 wvoff[2];
#pragma unroll
    for (int i = 0; i < 2; ++i) {
        const int r = 16 * wid + 8 * i + (lane >> 3), cc = (lane & 7) ^ ((r >> 1) & 7);
        wvoff[i] = (u32)(((64 * tp + r) * SL + cc * 8) * 2);
    }
    const char* z2base = ws + OFF_Z2T + (size_t)ftile * 128 * 1024;
    const u32 zvoff = lane * 16;
    u32x4 zf[G2_ZD][4];
    u32x4 z1v[2][2];
    u32 bsv[2];
    auto issue_w1 = [&](auto C, int i) LAM {
        constexpr int c = C.value, slot = c % G2_SLOTS;
        glds16((const char*)w_sp + KM(c) * 128, wvoff[i], lds0 + slot * 8192 + wid * 2048 + i * 1024);
    };
    static_for<0, G2_ZD>([&](auto C) LAM {
        constexpr int c = C.value;
        issue_w1(C, 0); issue_w1(C, 1);
        gload4x(zf[c][0], zf[c][1], zf[c][2], zf[c][3], zvoff, z2base + KM(c) * 4096);
    });
    f32x16 acc[2];
#pragma unroll
    for (int i = 0; i < 16; ++i) { acc[0][i] = 0.f; acc[1][i] = 0.f; }
    const int h = lane >> 5, tl = lane & 31;
    u32 roff[2][4];
#pragma unroll
    for (int tt = 0; tt < 2; ++tt)
#pragma unroll
        for (int kk = 0; kk < 4; ++kk) { const int r = 32 * tt + tl; roff[tt][kk] = r * 128 + (((2 * kk + h) ^ ((r >> 1) & 7)) * 16); }
    u32x4 wa[2][2], wb[2][2];
    auto read_w = [&](u32x4 (&dst)[2][2], int slot, int kk0) LAM {
#pragma unroll
        for (int kk = 0; kk < 2; ++kk)
#pragma unroll
            for (int tt = 0; tt < 2; ++tt) dst[kk][tt] = lds16(smem + slot * 8192 + roff[tt][kk0 + kk]);
    };
    wait_vm<6 * G2_ZD - 2>();
    bar();
    read_w(wa, 0, 0);
    STAMP(1, 1);
    static_for<0, 32>([&](auto C) LAM {
        constexpr int c = C.value, set = c % G2_ZD;
        wait_vm_tie4<g2_younger(c)>(zf[set][0], zf[set][1], zf[set][2], zf[set][3]);
        if constexpr (c == 8) STAMP(1, 2);
        if constexpr (c == 16) STAMP(1, 3);
        if constexpr (c == 24) STAMP(1, 4);
        lbar();
        constexpr bool more_w = c + G2_SLOTS - 1 < 32;
        constexpr bool more_z = c + G2_ZD < 32;
        const char* zn = z2base + KM(c + G2_ZD) * 4096;
        if constexpr (more_w) { issue_w1(IC<c + G2_SLOTS - 1>{}, 0); issue_w1(IC<c + G2_SLOTS - 1>{}, 1); }
        read_w(wb, c % G2_SLOTS, 2);
        sched_fence();
#define G2M(kk, tt, W) acc[tt] = mfma32_bf(zf[set][kk], W, acc[tt])
        G2M(0, 0, wa[0][0]);
        G2M(0, 1, wa[0][1]);
        sched_fence();
        if constexpr (more_z) gload1(zf[set][0], zvoff, zn);
        G2M(1, 0, wa[1][0]);
        G2M(1, 1, wa[1][1]);
        sched_fence();
        if constexpr (more_z) gload1(zf[set][1], zvoff, zn + 1024);
        if constexpr (c + 1 < 32) read_w(wa, (c + 1) % G2_SLOTS, 0);
        sched_fence();
        G2M(2, 0, wb[0][0]);
        G2M(2, 1, wb[0][1]);
        G2M(3, 0, wb[1][0]);
        G2M(3, 1, wb[1][1]);
        sched_fence();
        if constexpr (more_z) { gload1(zf[set][2], zvoff, zn + 2048); gload1(zf[set][3], zvoff, zn + 3072); }
        if constexpr (c == 27) {
            const char* z1 = ws + OFF_Z1 + (size_t)(ftile * 64 + 2 * tp) * 2048;
            gload4x(z1v[0][0], z1v[0][1], z1v[1][0], z1v[1][1], zvoff, z1);
            gload_u16x2(bsv[0], bsv[1], (u32)((64 * tp + (lane & 31)) * 2), (const char*)b_sp);
        }
    });
    STAMP(1, 5);
    asm volatile("s_waitcnt vmcnt(0)\n\ts_nop 7\n\ts_nop 7" : "+v"(acc[0]), "+v"(acc[1]), "+v"(z1v[0][0]), "+v"(z1v[0][1]), "+v"(z1v[1][0]), "+v"(z1v[1][1]), "+v"(bsv[0]), "+v"(bsv[1]));
    const char* wo = ws + OFF_WO + (size_t)((2 * j * 28 + 7 * wid) * 2) * 1024;
    const char* htp = ws + OFF_HT + (size_t)((2 * tp * 28 + 7 * wid) * 2) * 1024;
    bar();
    const u32 aring = lds0 + wid * (G3_AD * 2048);
    auto issue_a = [&](auto K) LAM {
        constexpr int k = K.value, slot = k % G3_AD;
        glds16(wo + k * 1024, zvoff, aring + slot * 2048);
        glds16(wo + (56 + k) * 1024, zvoff, aring + slot * 2048 + 1024);
    };
    static_for<0, G3_AD>(issue_a);
    char* ht = ws + OFF_HT;
#pragma unroll
    for (int tt = 0; tt < 2; ++tt) {
        const float bs = bf1((u16)bsv[tt]);
#pragma unroll
        for (int s = 0; s < 2; ++s) {
            u32x4 v;
#pragma unroll
            for (int k = 0; k < 4; ++k) {
                const u32 g = pkbf(acc[tt][8 * s + 2 * k], acc[tt][8 * s + 2 * k + 1]);
                const u32 g2 = pkbf(bflo(g) + bs, bfhi(g) + bs);
                const u32 z = z1v[tt][s][k];
                v[k] = pkbf(bflo(z) * bflo(g2), bfhi(z) * bfhi(g2));
            }
            gst16_dev(ht + ((size_t)(((2 * tp + tt) * 28 + ftile) * 2 + s) * 64 + lane) * 16, v);
        }
    }
    wait_vm<0>();
    u32* fl = (u32*)(ws + OFF_FL);
    if (lane == 0) __hip_atomic_store(fl + tp * 32 + ftile, 1u, __ATOMIC_RELAXED, __HIP_MEMORY_SCOPE_AGENT);
    STAMP(1, 6);
    for (int spin = 0; spin < (1 << 22); ++spin) {
        const u32 fh = __hip_atomic_load(fl + tp * 32 + 7 * wid + (lane < 7 ? lane : 0), __ATOMIC_RELAXED, __HIP_MEMORY_SCOPE_AGENT);
        if (__all(fh == 1u)) break;
        asm volatile("s_sleep 1" ::: "memory");
    }
    STAMP(1, 7);
    u32x4 gb[G3_BD][2], ga[2][2];
    static_for<0, G3_BD>([&](auto K) LAM {
        constexpr int k = K.value;
        gload1_dev(gb[k][0], zvoff, htp + k * 1024); gload1_dev(gb[k][1], zvoff, htp + (56 + k) * 1024);
    });
    f32x16 a3[2][2];
#pragma unroll
    for (int a = 0; a < 2; ++a)
#pragma unroll
        for (int c = 0; c < 2; ++c)
#pragma unroll
            for (int i = 0; i < 16; ++i) a3[a][c][i] = 0.f;
    const int qa = wid >> 1, qc = wid & 1;
    const int t3 = 64 * tp + 32 * qc + tl, d0 = 64 * j + 32 * qa + 4 * h;
    u32x2 xv[4];
    u32 sv[16];
    u32 sflag;
    const char* ard = smem + wid * (G3_AD * 2048) + lane * 16;
    ga[0][0] = lds16(ard); ga[0][1] = lds16(ard + 1024);
    static_for<0, 14>([&](auto K) LAM {
        constexpr int k = K.value, r = k % G3_BD, cur = k & 1;
        wait_vm_tie2<g3_younger(k)>(gb[r][0], gb[r][1]);
        if constexpr (k + 1 < 14) { ga[cur ^ 1][0] = lds16(ard + ((k + 1) % G3_AD) * 2048); ga[cur ^ 1][1] = lds16(ard + ((k + 1) % G3_AD) * 2048 + 1024); }
        sched_fence();
        a3[0][0] = mfma32_bf(ga[cur][0], gb[r][0], a3[0][0]);
        a3[0][1] = mfma32_bf(ga[cur][0], gb[r][1], a3[0][1]);
        a3[1][0] = mfma32_bf(ga[cur][1], gb[r][0], a3[1][0]);
        a3[1][1] = mfma32_bf(ga[cur][1], gb[r][1], a3[1][1]);
        sched_fence();
        if constexpr (k + G3_AD < 14) issue_a(IC<k + G3_AD>{});
        if constexpr (k + G3_BD < 14) {
            gload1_dev(gb[r][0], zvoff, htp + (k + G3_BD) * 1024); gload1_dev(gb[r][1], zvoff, htp + (56 + k + G3_BD) * 1024);
        }
        if constexpr (k == 3) gload_flag(sflag, (u32)((FL_S + 64 * j + 32 * qa + (lane & 31)) * 4), (const char*)fl);
        if constexpr (k == 7) {
            wait_vm_tie1<10>(sflag);
            if (!__all(sflag == 1u)) {
                for (int spin = 0; spin < (1 << 22); ++spin) {
                    const u32 fs = __hip_atomic_load(fl + FL_S + 64 * j + 32 * qa + (lane & 31), __ATOMIC_RELAXED, __HIP_MEMORY_SCOPE_AGENT);
                    if (__all(fs == 1u)) break;
                    asm volatile("s_sleep 1" ::: "memory");
                }
            }
            const u32 xo = (u32)((t3 * DM + d0) * 2), so = (u32)(((tp * DM + d0) * 64 + (t3 & 63)) * 2);
            gload_x4(xv, xo, (const char*)x);
            gload_s16(sv, so, ws + OFF_ST);
        }
    });
    STAMP(1, 8);
    asm volatile("s_waitcnt vmcnt(0)\n\ts_nop 7\n\ts_nop 7"
                 : "+v"(a3[0][0]), "+v"(a3[0][1]), "+v"(a3[1][0]), "+v"(a3[1][1]), "+v"(xv[0]), "+v"(xv[1]), "+v"(xv[2]), "+v"(xv[3]), "+v"(sv[0]),
                   "+v"(sv[1]), "+v"(sv[2]), "+v"(sv[3]), "+v"(sv[4]), "+v"(sv[5]), "+v"(sv[6]), "+v"(sv[7]), "+v"(sv[8]), "+v"(sv[9]),
                   "+v"(sv[10]), "+v"(sv[11]), "+v"(sv[12]), "+v"(sv[13]), "+v"(sv[14]), "+v"(sv[15])::"memory");
    lbar();
    f32x16 own;
#pragma unroll
    for (int q = 0; q < 4; ++q) {
        const f32x16& v = a3[q >> 1][q & 1];
        if (q == wid) { own = v; continue; }
        char* dst = smem + (q * 3 + ((wid - q - 1) & 3)) * 4096 + lane * 16;
#pragma unroll
        for (int g = 0; g < 4; ++g) *(f32x4*)(dst + g * 1024) = (f32x4){v[4 * g], v[4 * g + 1], v[4 * g + 2], v[4 * g + 3]};
    }
    lbar();
    STAMP(1, 9);
    float o[16];
    {
        f32x4 part[3][4];
#pragma unroll
        for (int sidx = 0; sidx < 3; ++sidx)
#pragma unroll
            for (int g = 0; g < 4; ++g) part[sidx][g] = *(const f32x4*)(smem + (wid * 3 + sidx) * 4096 + g * 1024 + lane * 16);
#pragma unroll
        for (int g = 0; g < 4; ++g)
#pragma unroll
            for (int i = 0; i < 4; ++i) o[4 * g + i] = (own[4 * g + i] + part[1][g][i]) + (part[0][g][i] + part[2][g][i]);
    }
    u32x2 ov[4];
#pragma unroll
    for (int g = 0; g < 4; ++g) {
        const u32 c0 = pkbf(o[4 * g], o[4 * g + 1]), c1 = pkbf(o[4 * g + 2], o[4 * g + 3]);
        float g0 = bflo(c0), g1 = bfhi(c0), g2 = bflo(c1), g3 = bfhi(c1);
        silu4(g0, g1, g2, g3);
        const u32 q0 = pkbf(g0, g1), q1 = pkbf(g2, g3);
        const u32 p0 = pkbf(bflo(sv[4 * g]) * bflo(q0), bflo(sv[4 * g + 1]) * bfhi(q0));
        const u32 p1 = pkbf(bflo(sv[4 * g + 2]) * bflo(q1), bflo(sv[4 * g + 3]) * bfhi(q1));
        ov[g][0] = pkbf(bflo(xv[g][0]) + bflo(p0), bfhi(xv[g][0]) + bfhi(p0));
        ov[g][1] = pkbf(bflo(xv[g][1]) + bflo(p1), bfhi(xv[g][1]) + bfhi(p1));
    }
#pragma unroll
    for (int g = 0; g < 4; g += 2) {
        auto s0 = __builtin_amdgcn_permlane32_swap(ov[g][0], ov[g + 1][0], false, false);
        auto s1 = __builtin_amdgcn_permlane32_swap(ov[g][1], ov[g + 1][1], false, false);
        gst16(out + (size_t)t3 * DM + 64 * j + 32 * qa + 8 * g + 8 * h, (u32x4){(u32)s0[0], (u32)s1[0], (u32)s0[1], (u32)s1[1]});
    }
    STAMP(1, 14);
}

template <int NS> DI void conv_seg(const char* smem, const char* kr, int lane, int tt, int e0, f32x4 (&acc)[4]) {
    constexpr int PD = 4;
    const int q = lane & 15, kg = lane >> 4, c = lane & 7, pl = (lane >> 3) & 1;
    const int m0 = -(32 * tt + 16) + e0;
    const char* ap = kr + (q - kg + 64) * 16 - 64 * m0 - 64 * (NS - 1);
    const char* bp = smem + c * CV_CS + (64 * (2 * tt + pl) + kg + 64) * 16 + 64 * m0;
    u32x4 F[NS + 12], B[NS];
#pragma unroll
    for (int n = 0; n < 12 + PD; ++n) F[n] = lds16(ap + 64 * (NS + 11 - n));
#pragma unroll
    for (int e = 0; e < PD; ++e) B[e] = lds16(bp + 64 * e);
#pragma unroll
    for (int a = 0; a < 4; ++a) acc[a] = (f32x4){0.f, 0.f, 0.f, 0.f};
    static_for<0, NS>([&](auto E) LAM {
        constexpr int e = E.value;
        if constexpr (e + PD < NS) { F[e + 12 + PD] = lds16(ap + 64 * (NS + 11 - (e + 12 + PD))); B[e + PD] = lds16(bp + 64 * (e + PD)); }
        sched_fence();
#pragma unroll
        for (int a = 0; a < 4; ++a) acc[a] = mfma16_h(F[e + 12 - 4 * a], B[e], acc[a]);
        sched_fence();
    });
}

DI void conv_ucopies(char* smem, int tid, u32x4 uprev, u32x4 ucur) {
    u32 v[8];
#pragma unroll
    for (int k = 0; k < 4; ++k) { v[k] = pkh(bflo(uprev[k]), bfhi(uprev[k])); v[4 + k] = pkh(bflo(ucur[k]), bfhi(ucur[k])); }
#pragma unroll
    for (int c = 0; c < 8; ++c) {
        u32x4 o;
#pragma unroll
        for (int k = 0; k < 4; ++k) {
            const int e = c + 1 + 2 * k;
            o[k] = (e & 1) ? __builtin_amdgcn_alignbyte(v[(e + 1) >> 1 < 8 ? (e + 1) >> 1 : 7], v[e >> 1], 2) : v[e >> 1];
        }
        *(u32x4*)(smem + c * CV_CS + (tid + 64) * 16) = o;
    }
}
DI void conv_run(const char* smem, const char* kr, int wid, int lane, f32x4 (&ca)[4], f32x4 (&cb)[4]) {
    if (wid == 0) conv_seg<24>(smem, kr, lane, 1, 0, ca);
    else if (wid == 1) conv_seg<24>(smem, kr, lane, 1, 24, ca);
    else if (wid == 2) { conv_seg<16>(smem, kr, lane, 1, 48, ca); conv_seg<8>(smem, kr, lane, 0, 0, cb); }
    else conv_seg<24>(smem, kr, lane, 0, 8, ca);
}
DI void conv_put(char* smem, int wid, int lane, const f32x4 (&ca)[4], const f32x4 (&cb)[4]) {
    const int kg = lane >> 4, c = lane & 7, pl = (lane >> 3) & 1;
    float* pb = (float*)(smem + CV_OFF_PB) + 512 * pl + c + 32 * kg;
    float* p0 = pb + 1024 * (wid == 3 ? 4 : wid);
#pragma unroll
    for (int a = 0; a < 4; ++a)
#pragma unroll
        for (int r = 0; r < 4; ++r) p0[128 * a + 8 * r] = ca[a][r];
    if (wid == 2) {
#pragma unroll
        for (int a = 0; a < 4; ++a)
#pragma unroll
            for (int r = 0; r < 4; ++r) pb[3 * 1024 + 128 * a + 8 * r] = cb[a][r];
    }
}
DI void conv_out(const char* smem, int tid, int d, float kinv, float dsk, u32x4 ucur, char* ws) {
    const char* yb = smem + CV_OFF_PB + ((8 * tid) & 1023) * 4;
    f32x4 y0, y1;
    if (tid < 128) {
        y0 = *(const f32x4*)(yb + 3 * 4096) + *(const f32x4*)(yb + 4 * 4096);
        y1 = *(const f32x4*)(yb + 3 * 4096 + 16) + *(const f32x4*)(yb + 4 * 4096 + 16);
    } else {
        y0 = (*(const f32x4*)(yb) + *(const f32x4*)(yb + 4096)) + *(const f32x4*)(yb + 2 * 4096);
        y1 = (*(const f32x4*)(yb + 16) + *(const f32x4*)(yb + 4096 + 16)) + *(const f32x4*)(yb + 2 * 4096 + 16);
    }
    u32x4 o;
#pragma unroll
    for (int k = 0; k < 4; ++k) {
        const float ya = k < 2 ? y0[2 * k] : y1[2 * k - 4], yb2 = k < 2 ? y0[2 * k + 1] : y1[2 * k - 3];
        const u32 y = pkbf(ya * kinv, yb2 * kinv);
        const u32 sk = pkbf(bflo(ucur[k]) * dsk, bfhi(ucur[k]) * dsk);
        o[k] = pkbf(bflo(y) + bflo(sk), bfhi(y) + bfhi(sk));
    }
    gst16_dev((u16*)(ws + OFF_ST) + ((size_t)(tid >> 3) * DM + d) * 64 + (tid & 7) * 8, o);
    wait_vm<0>();
    bar();
    if (tid == 0) __hip_atomic_store((u32*)(ws + OFF_FL) + FL_S + d, 1u, __ATOMIC_RELAXED, __HIP_MEMORY_SCOPE_AGENT);
}

template <bool GEN>
DI void conv_role(int d, char* smem, const u16* logA, const u16* Aim, const u16* Cre, const u16* Cim, const u16* logdt, const u16* Dsk,
                  char* ws) {
    const int tid = threadIdx.x, lane = tid & 63;
    const int wid = __builtin_amdgcn_readfirstlane(tid >> 6);
    STAMP_DECL;
    STAMP(1, 0);
    const u32 lds0 = (u32)(size_t)smem;
    KRaw pr;
    if constexpr (GEN) kgen_load(pr, d, tid, logA, Aim, Cre, Cim, logdt);
    else glds16(ws + OFF_KR + (size_t)d * 4096 + wid * 1024, lane * 16, lds0 + CV_OFF_KR + (64 + 64 * wid) * 16);
    const u16* xnt = (const u16*)(ws + OFF_XNT) + (size_t)d * SL;
    u32x4 ucur, uprev;
    u32 dskr, kir;
    {
        const u32 vo = tid * 16, vp = tid > 0 ? tid * 16 - 16 : 0;
        asm volatile("global_load_dwordx4 %0, %4, %6\n\tglobal_load_dwordx4 %1, %5, %6\n\tglobal_load_ushort %2, %7, %8\n\tglobal_load_dword %3, %7, %9"
                     : "=&v"(ucur), "=&v"(uprev), "=&v"(dskr), "=&v"(kir)
                     : "v"(vo), "v"(vp), "s"(xnt), "v"(0u), "s"(Dsk + d), "s"(ws + OFF_KI + d * 4)
                     : "memory");
    }
    if (tid < 64) *(u32x4*)(smem + CV_OFF_KR + tid * 16) = (u32x4){0u, 0u, 0u, 0u};
    float kinv = 0.f;
    if constexpr (GEN) {
        kgen_wait<4>(pr);
        kinv = kgen_wave_raw(smem + wid * CV_STG, lane, pr);
        lbar();
        kgen_reduce(smem, smem + 4 * CV_STG, tid);
        lbar();
        *(u32x4*)(smem + CV_OFF_KR + (tid + 64) * 16) = kgen_expand(smem + 4 * CV_STG, tid);
        lbar();
    }
#pragma unroll
    for (int i = 0; i < 2; ++i) {
        const int zc = tid + 256 * i;
        *(u32x4*)(smem + (zc >> 6) * CV_CS + (zc & 63) * 16) = (u32x4){0u, 0u, 0u, 0u};
    }
    asm volatile("s_waitcnt vmcnt(0)" : "+v"(ucur), "+v"(uprev), "+v"(dskr), "+v"(kir)::"memory");
    if constexpr (!GEN) kinv = __builtin_bit_cast(float, kir);
    if (tid == 0) uprev = (u32x4){0u, 0u, 0u, 0u};
    const float dsk = bf1((u16)dskr);
    conv_ucopies(smem, tid, uprev, ucur);
    STAMP(1, 3);
    lbar();
    STAMP(1, 4);
    f32x4 ca[4], cb[4];
    conv_run(smem, smem + CV_OFF_KR, wid, lane, ca, cb);
    STAMP(1, 5);
    lbar();
    conv_put(smem, wid, lane, ca, cb);
    lbar();
    STAMP(1, 6);
    conv_out(smem, tid, d, kinv, dsk, ucur, ws);
    STAMP(1, 14);
}

__global__ __launch_bounds__(256, 3) void k2_kernel(const u16* w_sp, const u16* b_sp, const u16* logA, const u16* Aim, const u16* Cre,
                                                    const u16* Cim, const u16* logdt, const u16* Dsk, const u16* x, u16* out, char* ws) {
    extern __shared__ __attribute__((aligned(16))) char smem[];
    const int b = blockIdx.x;
    if (b < 224) gemm2_role(b, smem, w_sp, b_sp, x, out, ws);
    if (b >= 224) {
        const int d = b - 224;
        if (kgen_in_k2(d)) conv_role<true>(d, smem, logA, Aim, Cre, Cim, logdt, Dsk, ws);
        else conv_role<false>(d, smem, logA, Aim, Cre, Cim, logdt, Dsk, ws);
    }
}

extern "C" void kernel_launch(void* const* d_in, const int* in_sizes, int n_in, void* d_out, int out_size, void* d_ws, size_t ws_size,
                              hipStream_t stream) {
    const u16* x = (const u16*)d_in[0];
    const u16* logA = (const u16*)d_in[1];
    const u16* Aim = (const u16*)d_in[2];
    const u16* Cre = (const u16*)d_in[3];
    const u16* Cim = (const u16*)d_in[4];
    const u16* logdt = (const u16*)d_in[5];
    const u16* Dsk = (const u16*)d_in[6];
    const u16* w_in = (const u16*)d_in[8];
    const u16* w_sp = (const u16*)d_in[9];
    const u16* b_sp = (const u16*)d_in[10];
    const u16* w_out = (const u16*)d_in[11];
    char* ws = (char*)d_ws;
    hipFuncSetAttribute((const void*)k1_kernel, hipFuncAttributeMaxDynamicSharedMemorySize, K1_LDS);
    hipFuncSetAttribute((const void*)k2_kernel, hipFuncAttributeMaxDynamicSharedMemorySize, K2_LDS);
    k1_kernel<<<256, 512, K1_LDS, stream>>>(x, w_in, w_out, w_sp, logA, Aim, Cre, Cim, logdt, ws);
    k2_kernel<<<224 + 448, 256, K2_LDS, stream>>>(w_sp, b_sp, logA, Aim, Cre, Cim, logdt, Dsk, x, (u16*)d_out, ws);
}
```

```cpp
#define LAM __attribute__((always_inline))
#include <hip/hip_runtime.h>
#include <hip/hip_bf16.h>
#include <stdint.h>

typedef unsigned short u16;
typedef unsigned int u32;
typedef unsigned long long u64;
#define LAS __attribute__((address_space(3)))
#define DI __device__ __forceinline__
typedef __attribute__((ext_vector_type(2))) __bf16 bf16x2;
typedef __attribute__((ext_vector_type(8))) __bf16 bf16x8;
typedef __attribute__((ext_vector_type(2))) _Float16 f16x2;
typedef __attribute__((ext_vector_type(8))) _Float16 f16x8;
typedef __attribute__((ext_vector_type(2))) float f32x2;
typedef __attribute__((ext_vector_type(4))) float f32x4;
typedef __attribute__((ext_vector_type(16))) float f32x16;
typedef __attribute__((ext_vector_type(4))) short s16x4;
typedef __attribute__((ext_vector_type(2))) u32 u32x2;
typedef __attribute__((ext_vector_type(4))) u32 u32x4;

constexpr int SL = 2048;
constexpr int DM = 448;
constexpr int FH = 896;
constexpr int NST = 256;

constexpr size_t OFF_XNT = 0;
constexpr size_t OFF_Z1 = OFF_XNT + (size_t)DM * SL * 2;
constexpr size_t OFF_Z2T = OFF_Z1 + (size_t)SL * FH * 2;
constexpr size_t OFF_HT = OFF_Z2T + (size_t)SL * FH * 2;
constexpr size_t OFF_WO = OFF_HT + (size_t)SL * FH * 2;
constexpr size_t OFF_ST = OFF_WO + (size_t)FH * DM * 2;
constexpr size_t OFF_GR = OFF_ST + (size_t)DM * SL * 2;
constexpr size_t OFF_EP = OFF_GR + (size_t)32 * 7 * 64 * 16;
constexpr size_t OFF_KR = OFF_EP + 32 * 128;
constexpr size_t OFF_KI = OFF_KR + (size_t)DM * 4096;
constexpr size_t OFF_FL = OFF_KI + 2048;
constexpr int FL_S = 1024;
constexpr int FL_WORDS = FL_S + 448;

#define STAMP(kern, i) do {} while (0)
#define STAMP_DECL do {} while (0)
template <int I> struct IC { static constexpr int value = I; };
template <int B, int E, class F> DI void static_for(F&& f) {
    if constexpr (B < E) { f(IC<B>{}); static_for<B + 1, E>(f); }
}
DI float bflo(u32 w) { return __builtin_bit_cast(float, w << 16); }
DI float bfhi(u32 w) { return __builtin_bit_cast(float, w & 0xffff0000u); }
DI float bf1(u16 h) { return __builtin_bit_cast(float, (u32)h << 16); }
DI u32 pkbf(float a, float b) { f32x2 v = {a, b}; bf16x2 r = __builtin_convertvector(v, bf16x2); return __builtin_bit_cast(u32, r); }
DI u32 pkh(float a, float b) { f32x2 v = {a, b}; f16x2 r = __builtin_convertvector(v, f16x2); return __builtin_bit_cast(u32, r); }
DI float rbf(float a) { return bflo(pkbf(a, a)); }
template <int CTRL> DI float dppf(float v) {
    return __builtin_bit_cast(float, __builtin_amdgcn_update_dpp(0, __builtin_bit_cast(int, v), CTRL, 0xF, 0xF, true));
}
DI float red8(float v) { v += dppf<0xB1>(v); v += dppf<0x4E>(v); v += dppf<0x141>(v); return v; }
DI float addx32(float v) {
    u32 b = __builtin_bit_cast(u32, v);
    auto r = __builtin_amdgcn_permlane32_swap(b, b, false, false);
    return __builtin_bit_cast(float, (u32)r[0]) + __builtin_bit_cast(float, (u32)r[1]);
}
DI f32x16 mfma32_bf(u32x4 a, u32x4 b, f32x16 c) {
    return __builtin_amdgcn_mfma_f32_32x32x16_bf16(__builtin_bit_cast(bf16x8, a), __builtin_bit_cast(bf16x8, b), c, 0, 0, 0);
}
DI f32x16 mfma32_h(u32x4 a, u32x4 b, f32x16 c) {
    return __builtin_amdgcn_mfma_f32_32x32x16_f16(__builtin_bit_cast(f16x8, a), __builtin_bit_cast(f16x8, b), c, 0, 0, 0);
}
DI f32x4 mfma16_h(u32x4 a, u32x4 b, f32x4 c) {
    return __builtin_amdgcn_mfma_f32_16x16x32_f16(__builtin_bit_cast(f16x8, a), __builtin_bit_cast(f16x8, b), c, 0, 0, 0);
}
DI u32x2 tr_read(const char* p) {
    s16x4 v = __builtin_amdgcn_ds_read_tr16_b64_v4i16((LAS s16x4*)p);
    return __builtin_bit_cast(u32x2, v);
}
DI u32x4 lds16(const char* p) { return *(const u32x4*)p; }
DI void glds16(const void* sbase, u32 voff, u32 lds_addr) {
    asm volatile("s_mov_b32 m0, %2\n\ts_nop 0\n\tglobal_load_lds_dwordx4 %0, %1" ::"v"(voff), "s"(sbase), "s"(lds_addr) : "memory");
}
DI void glds4(const void* sbase, u32 voff, u32 lds_addr) {
    asm volatile("s_mov_b32 m0, %2\n\ts_nop 0\n\tglobal_load_lds_dword %0, %1" ::"v"(voff), "s"(sbase), "s"(lds_addr) : "memory");
}
template <int N> DI void wait_vm() { asm volatile("s_waitcnt vmcnt(%0)" ::"n"(N) : "memory"); }
DI void gload4x(u32x4& a, u32x4& b, u32x4& c, u32x4& d, u32 voff, const char* base) {
    asm volatile("global_load_dwordx4 %0, %4, %5\n\t"
                 "global_load_dwordx4 %1, %4, %5 offset:1024\n\t"
                 "global_load_dwordx4 %2, %4, %5 offset:2048\n\t"
                 "global_load_dwordx4 %3, %4, %5 offset:3072"
                 : "=&v"(a), "=&v"(b), "=&v"(c), "=&v"(d)
                 : "v"(voff), "s"(base)
                 : "memory");
}
DI void gload1(u32x4& a, u32 voff, const char* base) {
    asm volatile("global_load_dwordx4 %0, %1, %2" : "=&v"(a) : "v"(voff), "s"(base) : "memory");
}
DI void gload_u16(u32& a, u32 voff, const char* base) {
    asm volatile("global_load_ushort %0, %1, %2" : "=&v"(a) : "v"(voff), "s"(base) : "memory");
}
DI void gload_u16x2(u32& a, u32& b, u32 voff, const char* base) {
    asm volatile("global_load_ushort %0, %2, %3\n\tglobal_load_ushort %1, %2, %3 offset:64" : "=&v"(a), "=&v"(b) : "v"(voff), "s"(base) : "memory");
}
DI void gload_x4(u32x2 (&v)[4], u32 voff, const char* base) {
    asm volatile("global_load_dwordx2 %0, %4, %5\n\tglobal_load_dwordx2 %1, %4, %5 offset:16\n\t"
                 "global_load_dwordx2 %2, %4, %5 offset:32\n\tglobal_load_dwordx2 %3, %4, %5 offset:48"
                 : "=&v"(v[0]), "=&v"(v[1]), "=&v"(v[2]), "=&v"(v[3]) : "v"(voff), "s"(base) : "memory");
}
DI void gload_s16(u32 (&v)[16], u32 voff, const char* base) {
#define S16L(n, off) "global_load_ushort %" #n ", %16, %17 offset:" #off " sc1\n\t"
    asm volatile(S16L(0, 0) S16L(1, 128) S16L(2, 256) S16L(3, 384) S16L(4, 1024) S16L(5, 1152) S16L(6, 1280) S16L(7, 1408)
                 S16L(8, 2048) S16L(9, 2176) S16L(10, 2304) S16L(11, 2432) S16L(12, 3072) S16L(13, 3200) S16L(14, 3328) S16L(15, 3456) "s_nop 0"
                 : "=&v"(v[0]), "=&v"(v[1]), "=&v"(v[2]), "=&v"(v[3]), "=&v"(v[4]), "=&v"(v[5]), "=&v"(v[6]), "=&v"(v[7]), "=&v"(v[8]),
                   "=&v"(v[9]), "=&v"(v[10]), "=&v"(v[11]), "=&v"(v[12]), "=&v"(v[13]), "=&v"(v[14]), "=&v"(v[15])
                 : "v"(voff), "s"(base) : "memory");
#undef S16L
}
DI void gload_flag(u32& v, u32 voff, const char* base) {
    asm volatile("global_load_dword %0, %1, %2 sc1" : "=&v"(v) : "v"(voff), "s"(base) : "memory");
}
template <int N> DI void wait_vm_tie1(u32& a) { asm volatile("s_waitcnt vmcnt(%1)" : "+v"(a) : "n"(N) : "memory"); }
template <int N> DI void wait_vm_tie2(u32x4& a, u32x4& b) {
    asm volatile("s_waitcnt vmcnt(%2)" : "+v"(a), "+v"(b) : "n"(N) : "memory");
}
template <int N> DI void wait_vm_tie4(u32x4& a, u32x4& b, u32x4& c, u32x4& d) {
    asm volatile("s_waitcnt vmcnt(%4)" : "+v"(a), "+v"(b), "+v"(c), "+v"(d) : "n"(N) : "memory");
}
#define GST_MODE 0
DI void gst16(void* p, u32x4 v) {
    *(u32x4*)p = v;
}
template <int M> DI void gst16m(void* p, u32x4 v) {
    if constexpr (M == 0) *(u32x4*)p = v;
    else if constexpr (M == 1) __builtin_nontemporal_store(v, (u32x4*)p);
    else asm volatile("global_store_dwordx4 %0, %1, off sc1\n\ts_nop 1" ::"v"(p), "v"(v) : "memory");
}
#define ST_EARLY 0
#define ST_LATE 0
#define ST_OUT 0
DI void gst16_dev(void* p, u32x4 v) {
    asm volatile("global_store_dwordx4 %0, %1, off sc1\n\ts_nop 1" ::"v"(p), "v"(v) : "memory");
}
DI void gload1_dev(u32x4& a, u32 voff, const char* base) {
    asm volatile("global_load_dwordx4 %0, %1, %2 sc1" : "=&v"(a) : "v"(voff), "s"(base) : "memory");
}
DI void wait_lgkm0() { asm volatile("s_waitcnt lgkmcnt(0)" ::: "memory"); }
DI void bar() { asm volatile("" ::: "memory"); __builtin_amdgcn_s_barrier(); asm volatile("" ::: "memory"); }
DI void lbar() { wait_lgkm0(); bar(); }
DI float dot2bf(u32 a, u32 b, float c) {
    return __builtin_amdgcn_fdot2_f32_bf16(__builtin_bit_cast(bf16x2, a), __builtin_bit_cast(bf16x2, b), c, false);
}
DI void sched_fence() { __builtin_amdgcn_sched_barrier(0); }
DI float gelu_tanh(float x) {
    const float c0 = -2.0f * 0.7978845608028654f * 1.4426950408889634f;
    const float c1 = c0 * 0.044715f;
    float t = __builtin_fmaf(x * x, c1, c0);
    float e = __builtin_amdgcn_exp2f(x * t);
    return x * __builtin_amdgcn_rcpf(1.0f + e);
}

DI float smul(float a, float b) { float r; asm("v_mul_f32 %0, %1, %2" : "=v"(r) : "v"(a), "v"(b)); return r; }
DI float sfma(float a, float b, float c) { float r; asm("v_fma_f32 %0, %1, %2, %3" : "=v"(r) : "v"(a), "v"(b), "v"(c)); return r; }
DI float sfnma(float a, float b, float c) { float r; asm("v_fma_f32 %0, -%1, %2, %3" : "=v"(r) : "v"(a), "v"(b), "v"(c)); return r; }
DI void gelu4(float& x0, float& x1, float& x2, float& x3) {
    const float c1 = -2.0f * 0.7978845608028654f * 1.4426950408889634f * 0.044715f;
    float t0, t1, t2, t3;
    asm("v_mul_f32 %4, %0, %0\n\tv_mul_f32 %5, %1, %1\n\tv_mul_f32 %6, %2, %2\n\tv_mul_f32 %7, %3, %3\n\t"
        "v_fmaak_f32 %4, %4, %8, 0xc0135761\n\tv_fmaak_f32 %5, %5, %8, 0xc0135761\n\tv_fmaak_f32 %6, %6, %8, 0xc0135761\n\tv_fmaak_f32 %7, %7, %8, 0xc0135761\n\t"
        "v_mul_f32 %4, %4, %0\n\tv_mul_f32 %5, %5, %1\n\tv_mul_f32 %6, %6, %2\n\tv_mul_f32 %7, %7, %3\n\t"
        "v_exp_f32 %4, %4\n\tv_exp_f32 %5, %5\n\tv_exp_f32 %6, %6\n\tv_exp_f32 %7, %7\n\t"
        "v_add_f32 %4, 1.0, %4\n\tv_add_f32 %5, 1.0, %5\n\tv_add_f32 %6, 1.0, %6\n\tv_add_f32 %7, 1.0, %7\n\t"
        "v_rcp_f32 %4, %4\n\tv_rcp_f32 %5, %5\n\tv_rcp_f32 %6, %6\n\tv_rcp_f32 %7, %7\n\t"
        "v_mul_f32 %0, %0, %4\n\tv_mul_f32 %1, %1, %5\n\tv_mul_f32 %2, %2, %6\n\tv_mul_f32 %3, %3, %7"
        : "+v"(x0), "+v"(x1), "+v"(x2), "+v"(x3), "=&v"(t0), "=&v"(t1), "=&v"(t2), "=&v"(t3)
        : "v"(c1));
}
DI f32x2 gelu2(f32x2 x) {
    const float c0 = -2.0f * 0.7978845608028654f * 1.4426950408889634f;
    const float c1 = c0 * 0.044715f;
    const f32x2 t = __builtin_elementwise_fma(x * x, (f32x2){c1, c1}, (f32x2){c0, c0}) * x;
    const f32x2 d = (f32x2){__builtin_amdgcn_exp2f(t[0]), __builtin_amdgcn_exp2f(t[1])} + 1.0f;
    return x * (f32x2){__builtin_amdgcn_rcpf(d[0]), __builtin_amdgcn_rcpf(d[1])};
}

constexpr int CV_STG = 32 * 272;
struct cpx { float re, im; };
template <int OFF0, int OFF1> DI void lds_put2(u32 addr, u32 a, u32 b) {
    asm volatile("ds_write2_b32 %0, %1, %2 offset0:%3 offset1:%4" ::"v"(addr), "v"(a), "v"(b), "n"(OFF0), "n"(OFF1) : "memory");
}
DI void cmul4(cpx (&o)[4], const cpx& a0, const cpx& b0, const cpx& a1, const cpx& b1, const cpx& a2, const cpx& b2, const cpx& a3,
              const cpx& b3) {
    asm("v_mul_f32 %0, %8, %10\n\tv_mul_f32 %2, %12, %14\n\tv_mul_f32 %4, %16, %18\n\tv_mul_f32 %6, %20, %22\n\t"
        "v_mul_f32 %1, %8, %11\n\tv_mul_f32 %3, %12, %15\n\tv_mul_f32 %5, %16, %19\n\tv_mul_f32 %7, %20, %23\n\t"
        "v_fma_f32 %0, -%9, %11, %0\n\tv_fma_f32 %2, -%13, %15, %2\n\tv_fma_f32 %4, -%17, %19, %4\n\tv_fma_f32 %6, -%21, %23, %6\n\t"
        "v_fma_f32 %1, %9, %10, %1\n\tv_fma_f32 %3, %13, %14, %3\n\tv_fma_f32 %5, %17, %18, %5\n\tv_fma_f32 %7, %21, %22, %7"
        : "=&v"(o[0].re), "=&v"(o[0].im), "=&v"(o[1].re), "=&v"(o[1].im), "=&v"(o[2].re), "=&v"(o[2].im), "=&v"(o[3].re), "=&v"(o[3].im)
        : "v"(a0.re), "v"(a0.im), "v"(b0.re), "v"(b0.im), "v"(a1.re), "v"(a1.im), "v"(b1.re), "v"(b1.im), "v"(a2.re), "v"(a2.im),
          "v"(b2.re), "v"(b2.im), "v"(a3.re), "v"(a3.im), "v"(b3.re), "v"(b3.im));
}
DI float exp_acc(float x) {
    const float t = x * 1.44269502f;
    const float r = __builtin_fmaf(x, 1.92596299e-8f, __builtin_fmaf(x, 1.44269502f, -t));
    const float e = __builtin_amdgcn_exp2f(t);
    return __builtin_fmaf(e, r * 0.693147182f, e);
}
DI float wave_sum(float v) {
    v = red8(v);
    v += dppf<0x128>(v);
    const int b = __builtin_bit_cast(int, v);
    return (__builtin_bit_cast(float, __builtin_amdgcn_readlane(b, 0)) + __builtin_bit_cast(float, __builtin_amdgcn_readlane(b, 16))) +
           (__builtin_bit_cast(float, __builtin_amdgcn_readlane(b, 32)) + __builtin_bit_cast(float, __builtin_amdgcn_readlane(b, 48)));
}
DI float kgen_wave(char* stg, int lane, float lar, float aim, float cr, float ci, float ldt, float cabs) {
    const float dt = rbf(exp_acc(ldt));
    const float are = -rbf(exp_acc(lar)) * dt, aimd = aim * dt;
    const float bound = wave_sum(cabs) * (2.0f * dt);
    int ex = (int)((__builtin_bit_cast(u32, bound) >> 23) & 0xff);
    ex = ex < 30 ? 30 : (ex > 230 ? 230 : ex);
    const float scl = __builtin_bit_cast(float, (u32)(127 + 14 + 126 - ex) << 23);
    const float a2 = are * 1.4426950408889634f;
    const float thi = aimd * 0.15915494309189535f;
    const float tlo = __builtin_fmaf(aimd, 0.15915494309189535f, -thi) + aimd * 6.4206383e-09f;
    auto cexp = [&](float pw) LAM {
        const float e = __builtin_amdgcn_exp2f(a2 * pw);
        const float th = __builtin_amdgcn_fractf(thi * pw) + tlo * pw;
        return cpx{e * __builtin_amdgcn_cosf(th), e * __builtin_amdgcn_sinf(th)};
    };
    const cpx E1 = cexp(1.0f), E8 = cexp(8.0f), E256 = cexp(256.0f);
    const u32 sa = (u32)(size_t)stg + lane * 4;
    auto pk = [&](const cpx& v) LAM { return pkh(v.re, v.im); };
    const cpx e1 = {E1.re, -E1.im}, g1 = {E256.re, -E256.im};
    const float cs2 = 2.0f * dt * scl;
    const cpx C = {cr * cs2, -ci * cs2};
    cpx t[4], y[4], u[4];
    cmul4(t, E8, E8, e1, e1, g1, g1, C, g1);
    const cpx E16 = t[0], e2 = t[1], g2 = t[2], c256 = t[3];
    cmul4(t, E16, E8, E16, E16, e2, e1, e2, e2);
    const cpx E32 = t[1], e3 = t[2], e4 = t[3];
    y[0] = cpx{1.0f, 0.0f}; y[1] = E8; y[2] = E16; y[3] = t[0];
    cmul4(t, g2, g1, g2, g2, C, g2, C, e1);
    const cpx g3 = t[0], g4 = t[1], c512 = t[2], r1 = t[3];
#pragma unroll
    for (int jj = 0; jj < 8; ++jj) {
        lds_put2<0, 68>(sa + jj * 1088, pk(y[0]), pk(y[1]));
        lds_put2<136, 204>(sa + jj * 1088, pk(y[2]), pk(y[3]));
        if (jj < 7) { cmul4(u, y[0], E32, y[1], E32, y[2], E32, y[3], E32); y[0] = u[0]; y[1] = u[1]; y[2] = u[2]; y[3] = u[3]; }
    }
    const int r = lane & 31, h = lane >> 5;
    u32x4 bfr[8];
#pragma unroll
    for (int kk = 0; kk < 8; ++kk) bfr[kk] = lds16(stg + r * 272 + (8 * kk + 4 * h) * 4);
    cpx e567[4], g5678[4];
    cmul4(e567, e4, e1, e4, e2, e4, e3, C, g3);
    cmul4(g5678, g4, g1, g4, g2, g4, g3, g4, g4);
    const cpx c768 = e567[3];
    lds_put2<0, 68>(sa, pk(C), pk(r1));
    cmul4(t, C, e2, C, e3, C, e4, C, e567[0]);
    lds_put2<136, 204>(sa, pk(t[0]), pk(t[1]));
    lds_put2<0, 68>(sa + 4 * 272, pk(t[2]), pk(t[3]));
    cmul4(t, C, e567[1], C, e567[2], c256, e2, c256, e4);
    lds_put2<136, 204>(sa + 4 * 272, pk(t[0]), pk(t[1]));
    lds_put2<0, 68>(sa + 8 * 272, pk(c256), pk(t[2]));
    cmul4(u, c256, e567[1], c512, e4, c768, e4, C, g4);
    lds_put2<136, 204>(sa + 8 * 272, pk(t[3]), pk(u[0]));
    lds_put2<0, 68>(sa + 12 * 272, pk(c512), pk(u[1]));
    lds_put2<136, 204>(sa + 12 * 272, pk(c768), pk(u[2]));
    cmul4(t, C, g5678[0], C, g5678[1], C, g5678[2], C, g5678[3]);
    lds_put2<0, 68>(sa + 16 * 272, pk(u[3]), pk(t[0]));
    lds_put2<136, 204>(sa + 16 * 272, pk(t[1]), pk(t[2]));
    *(u32*)(stg + 20 * 272 + lane * 4) = pk(t[3]);
    u32x4 afr[8];
#pragma unroll
    for (int kk = 0; kk < 8; ++kk) afr[kk] = lds16(stg + r * 272 + (8 * kk + 4 * h) * 4);
    sched_fence();
    f32x16 acc;
#pragma unroll
    for (int i = 0; i < 16; ++i) acc[i] = 0.f;
#pragma unroll
    for (int kk = 0; kk < 8; ++kk) acc = mfma32_h(afr[kk], bfr[kk], acc);
#pragma unroll
    for (int g = 0; g < 3; ++g)
        *(f32x4*)(stg + (g * 64 + lane) * 16) = (f32x4){acc[4 * g], acc[4 * g + 1], acc[4 * g + 2], acc[4 * g + 3]};
    return __builtin_bit_cast(float, (u32)(127 - 14 - 126 + ex) << 23);
}
DI void kgen_reduce(const char* pin, char* tout, int tid) {
    if (tid < 192) {
        const char* pp = pin + tid * 16;
        const f32x4 a = *(const f32x4*)pp, b = *(const f32x4*)(pp + CV_STG), c = *(const f32x4*)(pp + 2 * CV_STG), d = *(const f32x4*)(pp + 3 * CV_STG);
        *(f32x4*)(tout + tid * 16) = (a + b) + (c + d);
    }
}
DI float ktab(const char* tin, int idx) {
    const int row = idx >> 5, col = idx & 31;
    return *(const float*)(tin + (((row >> 3) * 64 + col + 32 * ((row >> 2) & 1)) * 4 + (row & 3)) * 4);
}
DI u32x4 kgen_expand(const char* tin, int z) {
    float kv[8];
    if (z < 32) {
#pragma unroll
        for (int e = 0; e < 8; ++e) kv[e] = ktab(tin, e * 32 + z);
    } else if (z < 64) {
        const int r = z - 32;
        const float v0 = ktab(tin, 256 + r), v2 = ktab(tin, 288 + r), v4 = ktab(tin, 320 + r), v6 = ktab(tin, 352 + r), v8 = ktab(tin, z == 63 ? 384 : 257 + r);
        kv[0] = v0; kv[1] = v0; kv[2] = v2; kv[3] = v4; kv[4] = v4; kv[5] = v4; kv[6] = v6; kv[7] = v8;
    } else if (z < 128) {
        const int r = z - 64, r1 = r + 1;
        const int i0 = (12 + 2 * (r >> 5)) * 32 + (r & 31), i8 = (12 + 2 * (r1 >> 5)) * 32 + (r1 & 31);
        const float v0 = ktab(tin, i0), v4 = ktab(tin, i0 + 32), v8 = ktab(tin, i8);
        kv[0] = v0; kv[1] = v0; kv[2] = v0; kv[3] = v4; kv[4] = v4; kv[5] = v4; kv[6] = v8; kv[7] = v8;
    } else {
        const float v0 = ktab(tin, z + 384), v8 = ktab(tin, z + 385);
        kv[0] = v0; kv[1] = v0; kv[2] = v0; kv[3] = v0; kv[4] = (z & 1) ? v8 : v0; kv[5] = v8; kv[6] = v8; kv[7] = v8;
    }
    return (u32x4){pkh(kv[7], kv[6]), pkh(kv[5], kv[4]), pkh(kv[3], kv[2]), pkh(kv[1], kv[0])};
}
struct KRaw { u32 v[11]; };
DI void kgen_load(KRaw& p, int d, int n, const u16* logA, const u16* Aim, const u16* Cre, const u16* Cim, const u16* logdt) {
    const u32 vo = (u32)((d * NST + n) * 2), v1 = (u32)((d * NST + (n ^ 64)) * 2), v2 = (u32)((d * NST + (n ^ 128)) * 2),
              v3 = (u32)((d * NST + (n ^ 192)) * 2), vd = (u32)(d * 2);
    asm volatile("global_load_ushort %0, %11, %16\n\tglobal_load_ushort %1, %11, %17\n\tglobal_load_ushort %2, %11, %18\n\t"
                 "global_load_ushort %3, %11, %19\n\tglobal_load_ushort %4, %15, %20\n\t"
                 "global_load_ushort %5, %12, %18\n\tglobal_load_ushort %6, %12, %19\n\tglobal_load_ushort %7, %13, %18\n\t"
                 "global_load_ushort %8, %13, %19\n\tglobal_load_ushort %9, %14, %18\n\tglobal_load_ushort %10, %14, %19"
                 : "=&v"(p.v[0]), "=&v"(p.v[1]), "=&v"(p.v[2]), "=&v"(p.v[3]), "=&v"(p.v[4]), "=&v"(p.v[5]), "=&v"(p.v[6]), "=&v"(p.v[7]),
                   "=&v"(p.v[8]), "=&v"(p.v[9]), "=&v"(p.v[10])
                 : "v"(vo), "v"(v1), "v"(v2), "v"(v3), "v"(vd), "s"(logA), "s"(Aim), "s"(Cre), "s"(Cim), "s"(logdt)
                 : "memory");
}
template <int N> DI void kgen_wait(KRaw& p) {
    asm volatile("s_waitcnt vmcnt(%11)"
                 : "+v"(p.v[0]), "+v"(p.v[1]), "+v"(p.v[2]), "+v"(p.v[3]), "+v"(p.v[4]), "+v"(p.v[5]), "+v"(p.v[6]), "+v"(p.v[7]), "+v"(p.v[8]),
                   "+v"(p.v[9]), "+v"(p.v[10])
                 : "n"(N) : "memory");
}
DI float kgen_wave_raw(char* stg, int lane, const KRaw& p) {
    float cabs = 0.f;
#pragma unroll
    for (int k = 0; k < 4; ++k) {
        const int ia = k == 0 ? 2 : 3 + 2 * k, ib = k == 0 ? 3 : 4 + 2 * k;
        cabs += __builtin_fabsf(bf1((u16)p.v[ia])) + __builtin_fabsf(bf1((u16)p.v[ib]));
    }
    return kgen_wave(stg, lane, bf1((u16)p.v[0]), bf1((u16)p.v[1]), bf1((u16)p.v[2]), bf1((u16)p.v[3]), bf1((u16)p.v[4]), cabs);
}
DI void soft_bar(char* cnt, int lane, u32 target) {
    wait_lgkm0();
    if (lane == 0) __hip_atomic_fetch_add((LAS u32*)cnt, 1u, __ATOMIC_RELAXED, __HIP_MEMORY_SCOPE_WORKGROUP);
    for (int spin = 0; spin < (1 << 22); ++spin) {
        if (__hip_atomic_load((LAS u32*)cnt, __ATOMIC_RELAXED, __HIP_MEMORY_SCOPE_WORKGROUP) >= target) break;
        asm volatile("s_sleep 1" ::: "memory");
    }
    asm volatile("" ::: "memory");
}

DI void soft_arrive(char* cnt, int lane) {
    wait_lgkm0();
    if (lane == 0) __hip_atomic_fetch_add((LAS u32*)cnt, 1u, __ATOMIC_RELAXED, __HIP_MEMORY_SCOPE_WORKGROUP);
}
DI void soft_wait(char* cnt, u32 target) {
    for (int spin = 0; spin < (1 << 22); ++spin) {
        if (__hip_atomic_load((LAS u32*)cnt, __ATOMIC_RELAXED, __HIP_MEMORY_SCOPE_WORKGROUP) >= target) break;
        asm volatile("s_sleep 1" ::: "memory");
    }
    asm volatile("" ::: "memory");
}

DI void tile_map(int b, int& panel, int& grp) {
    const int xcd = b & 7, idx = b >> 3, y = xcd >> 1, side = xcd & 1;
    int tpl;
    if (side == 0) {
        if (idx < 16) { tpl = idx >> 2; grp = idx & 3; }
        else { tpl = 4 + (idx - 16) / 3; grp = (idx - 16) % 3; }
    } else {
        if (idx < 12) { tpl = idx / 3; grp = 4 + idx % 3; }
        else { tpl = 4 + ((idx - 12) >> 2); grp = ((idx - 12) & 3) == 3 ? 3 : 4 + ((idx - 12) & 3); }
    }
    panel = 8 * y + tpl;
}
DI void l2_touch(u32& sink, const char* base, int lane) {
    asm volatile("global_load_dword %0, %1, %2" : "+v"(sink) : "v"(lane * 64), "s"(base) : "memory");
}

constexpr int CODE_TOUCH = 34816;
#define CODE_ADDR(dst, sym) do { unsigned long long pc_; int off_; \
    asm volatile("s_getpc_b64 %0\n1:\n\ts_mov_b32 %1, " #sym "-1b" : "=s"(pc_), "=s"(off_)); dst = (const char*)pc_ + (long long)off_; } while (0)
DI void code_touch(const char* base, int piece, int lane, u32 sink_lds) {
    const int line = piece * 64 + lane;
    glds4(base, (u32)((line * 128 < CODE_TOUCH ? line : piece * 64) * 128), sink_lds);
}

constexpr int K1_XSTRIDE = 912;
#define K1R 8
constexpr int K1_RING = K1R;
constexpr int K1_EARLY = 2;
constexpr int K1_OFF_RING = 64 * K1_XSTRIDE;
constexpr int K1_PS_IN_RING = 8192;
constexpr int K1_OFF_ST = K1_OFF_RING + 4 * K1_RING * 2048;
constexpr int K1_OFF_CNT = K1_OFF_ST + 256;
constexpr int KG_BYTES = 4 * CV_STG + 2 * 3072;
constexpr int K1_OFF_SINK = K1_OFF_CNT + 32;
constexpr int K1_OFF_KG = K1_OFF_SINK + 256;
constexpr int K1_LDS = K1_OFF_KG + 4 * CV_STG + 3072;
static_assert(K1_LDS <= 163840 && 2 * KG_BYTES + 64 + 256 <= K1_LDS && K1_RING * 2048 >= K1_PS_IN_RING + 512, "LDS");
DI bool kgen_in_k2(int d) { return d < 32 || (d >= 256 && d < 288); }
DI int kgen_k1_channel(int q) { return q < 224 ? 32 + q : 64 + q; }
DI void kgen_channel(char* kgb, char* cnt, int round, const KRaw& pr, int ft, int lane, int d, char* ws) {
    const int n = ft * 64 + lane;
    char* T = kgb + 4 * CV_STG + (round & 1) * 3072;
    const float kinv = kgen_wave_raw(kgb + ft * CV_STG, lane, pr);
    soft_bar(cnt, lane, 4 * (2 * round + 1));
    kgen_reduce(kgb, T, n);
    soft_bar(cnt, lane, 4 * (2 * round + 2));
    gst16m<ST_EARLY>(ws + OFF_KR + (size_t)d * 4096 + n * 16, kgen_expand(T, n));
    if (n == 0) *(float*)(ws + OFF_KI + d * 4) = kinv;
}
constexpr u32 TAG_C = 0x9E3779B9u, TAG_X = 0x5BD1E995u;

extern "C" __global__ __launch_bounds__(512, 2) void k1_kernel(const u16* x, const u16* w_in, const u16* w_out, const u16* w_sp, const u16* b_sp, const u16* logA,
                                                    const u16* Aim, const u16* Cre, const u16* Cim, const u16* logdt, char* ws) {
    extern __shared__ __attribute__((aligned(16))) char smem[];
    const int tid = threadIdx.x, lane = tid & 63;
    const int wid = __builtin_amdgcn_readfirstlane(tid >> 6);
    const int b = blockIdx.x;
    if (b >= 224) {
        const int a = b - 224, grp = wid >> 2, ftk = wid & 3, nch = 3 - grp, q0 = 224 + 5 * a + 3 * grp;
        char* cnt = smem + grp * 16;
        char* kgb = smem + 64 + grp * KG_BYTES;
        STAMP_DECL;
        STAMP(0, 0);
        if (a == 8 && wid == 0) glds4(b_sp, (lane & 31) * 128, (u32)(size_t)smem + 2 * KG_BYTES + 64);
        if (a < 16 && wid < 5) {
            const char* cb;
            if (a < 8) CODE_ADDR(cb, k1_kernel); else CODE_ADDR(cb, k2_kernel);
            code_touch(cb, wid, lane, (u32)(size_t)smem + 2 * KG_BYTES + 64);
        }
        KRaw pr[3];
#pragma unroll
        for (int c = 0; c < 3; ++c) kgen_load(pr[c], kgen_k1_channel(q0 + (c < nch ? c : 0)), ftk * 64 + lane, logA, Aim, Cre, Cim, logdt);
        const int u = a * 256 + (tid & 255);
        const bool do_wo = grp == 1 && u < 28 * 2 * 2 * 56;
        const int uu = do_wo ? u : 0, o = uu % 56, r = uu / 56, hh = r & 1, sk = (r >> 1) & 1, ftw = r >> 2;
        u32x4 rows[8];
        {
            const u32 vo = (u32)(((32 * ftw + 16 * sk + 4 * hh) * DM + 8 * o) * 2);
            asm volatile("global_load_dwordx4 %0, %8, %9\n\tglobal_load_dwordx4 %1, %8, %9 offset:896\n\t"
                         "global_load_dwordx4 %2, %8, %9 offset:1792\n\tglobal_load_dwordx4 %3, %8, %9 offset:2688\n\t"
                         "global_load_dwordx4 %4, %10, %9\n\tglobal_load_dwordx4 %5, %10, %9 offset:896\n\t"
                         "global_load_dwordx4 %6, %10, %9 offset:1792\n\tglobal_load_dwordx4 %7, %10, %9 offset:2688"
                         : "=&v"(rows[0]), "=&v"(rows[1]), "=&v"(rows[2]), "=&v"(rows[3]), "=&v"(rows[4]), "=&v"(rows[5]), "=&v"(rows[6]), "=&v"(rows[7])
                         : "v"(vo), "s"(w_out), "v"(vo + 8 * DM * 2)
                         : "memory");
        }
        if (tid < 8) *(u32*)(smem + tid * 4) = 0;
        if (a == 0) for (int i = tid; i < FL_WORDS; i += 512) *(u32*)(ws + OFF_FL + i * 4) = 0;
        lbar();
#pragma unroll
        for (int c = 0; c < 3; ++c) kgen_wait<0>(pr[c]);
        wait_vm_tie4<0>(rows[0], rows[1], rows[2], rows[3]);
        wait_vm_tie4<0>(rows[4], rows[5], rows[6], rows[7]);
        if (do_wo) {
            char* wo = ws + OFF_WO;
#pragma unroll
            for (int dd = 0; dd < 8; ++dd) {
                u32x4 v;
#pragma unroll
                for (int k = 0; k < 4; ++k) {
                    const u32 ra = rows[2 * k][dd >> 1], rb = rows[2 * k + 1][dd >> 1];
                    v[k] = (dd & 1) ? ((ra >> 16) | (rb & 0xffff0000u)) : ((ra & 0xffffu) | (rb << 16));
                }
                const int d = 8 * o + dd, dt = d >> 5, dl = d & 31;
                gst16m<ST_EARLY>(wo + ((size_t)(((dt * 28 + ftw) * 2 + sk) * 64 + dl + 32 * hh)) * 16, v);
            }
        }
        u32 sink = 0;
        auto prefetch_wsp = [&](int r) LAM {
        };
        prefetch_wsp(0);
        prefetch_wsp(1);
        STAMP(0, 1);
#pragma unroll
        for (int c = 0; c < 3; ++c) {
            if (c < nch) kgen_channel(kgb, cnt, c, pr[c], ftk, lane, kgen_k1_channel(q0 + c), ws);
            if (c < 2) prefetch_wsp(c + 2);
            if (c == 0) STAMP(0, 2);
            if (c == 1) STAMP(0, 3);
            if (c == 2) STAMP(0, 4);
        }
        asm volatile("s_waitcnt vmcnt(0)" : "+v"(sink)::"memory");
        STAMP(0, 14);
        return;
    }
    int p, j;
    tile_map(b, p, j);
    const u32 lds0 = (u32)(size_t)smem;
    STAMP_DECL;
    STAMP(0, 0);
    const int ft = wid & 3;
    auto load_x = [&]() LAM {
        if (lane < 56) {
            const char* xb = (const char*)x + (size_t)(64 * p + 8 * wid) * (DM * 2);
#pragma unroll
            for (int i = 0; i < 8; ++i) glds16(xb + i * (DM * 2), lane * 16, lds0 + (8 * wid + i) * K1_XSTRIDE);
        }
    };
    auto center_x = [&]() LAM {
        const int row = 8 * wid + (lane >> 3), t8 = lane & 7;
        char* xp = smem + row * K1_XSTRIDE + t8 * 16;
        float s0 = 0.f, s1 = 0.f, q0 = 0.f, q1 = 0.f;
        u32x4 xr[7];
#pragma unroll
        for (int i = 0; i < 7; ++i) xr[i] = lds16(xp + i * 128);
        sched_fence();
#pragma unroll
        for (int i = 0; i < 7; ++i) {
            const u32x4 v = xr[i];
            s0 = dot2bf(v[0], 0x3F803F80u, s0); s1 = dot2bf(v[1], 0x3F803F80u, s1);
            q0 = dot2bf(v[0], v[0], q0); q1 = dot2bf(v[1], v[1], q1);
            s0 = dot2bf(v[2], 0x3F803F80u, s0); s1 = dot2bf(v[3], 0x3F803F80u, s1);
            q0 = dot2bf(v[2], v[2], q0); q1 = dot2bf(v[3], v[3], q1);
        }
        const float mean = red8(s0 + s1) * (1.0f / DM);
        const float var = __builtin_fmaxf(red8(q0 + q1) * (1.0f / DM) - mean * mean, 0.f);
        const float mb = rbf(mean);
#pragma unroll
        for (int i = 0; i < 7; ++i) {
            u32x4 o;
#pragma unroll
            for (int k = 0; k < 4; ++k) o[k] = pkbf(bflo(xr[i][k]) - mb, bfhi(xr[i][k]) - mb);
            *(u32x4*)(xp + i * 128) = o;
        }
        const float sd = rbf(__builtin_amdgcn_sqrtf(rbf(rbf(var) + 1e-5f)));
        if (t8 == 0) *(float*)(smem + K1_OFF_ST + row * 4) = __builtin_amdgcn_rcpf(sd);
    };

    const u32 wvoff = (u32)(((lane >> 2) * (2 * FH) + 128 * j + 32 * ft + (lane & 3) * 8) * 2);
    const u32 ring = lds0 + K1_OFF_RING + ft * (K1_RING * 2048);
    auto issue_w1 = [&](auto I, int z1) LAM {
        constexpr int ks = I.value, slot = ks % K1_RING;
        glds16((const char*)w_in + (size_t)ks * 16 * (2 * FH) * 2 + (z1 ? 0 : FH * 2), wvoff, ring + slot * 2048 + z1 * 1024);
    };
    auto issue_w = [&](auto I) LAM { issue_w1(I, 0); issue_w1(I, 1); };
    if (wid >= 4) {
        load_x();
        KRaw pr;
        kgen_load(pr, kgen_k1_channel(b), ft * 64 + lane, logA, Aim, Cre, Cim, logdt);
        {
            const int nsh = j == 3 ? 4 : 8, sh = j == 3 ? (p & 3) : (p & 7), per = 448 / nsh;
#pragma unroll
            for (int r = 0; r < 2; ++r) {
                const int k = 64 * r + lane < per ? 64 * r + lane : 0, line = (sh * 4 + ft) * per + k;
                glds4(w_in, (u32)(((line >> 2) * (2 * FH) + ((line & 2) ? 0 : FH) + 128 * j + (line & 1) * 64) * 2), lds0 + K1_OFF_SINK);
            }
        }
        constexpr int NTOUCH = 2;
        constexpr int NT2 = 0;
        bar();
        __builtin_amdgcn_s_setprio(2);
        wait_vm<11 + NTOUCH + NT2>();
        center_x();
        soft_arrive(smem + K1_OFF_CNT + 4, lane);
        kgen_wait<0>(pr);
#pragma unroll
        for (int r = 0; r < 2; ++r) {
            int piece = (b * 4 + ft) * 2 + r;
            piece = piece < 1024 ? piece : 1023;
            glds4((const char*)w_sp + ((size_t)piece << 13), lane * 128, lds0 + K1_OFF_SINK);
        }
        __builtin_amdgcn_s_setprio(0);
        kgen_channel(smem + K1_OFF_KG, smem + K1_OFF_CNT + 8, 0, pr, ft, lane, kgen_k1_channel(b), ws);
        STAMP(0, 14);
        return;
    }
    u32 epoch;
    {
        const char* ep = ws + OFF_EP + p * 128;
        const u32 zero = 0;
        asm volatile("global_load_dword %0, %1, %2 sc1" : "=&v"(epoch) : "v"(zero), "s"(ep) : "memory");
    }
    load_x();
    static_for<0, K1_EARLY>(issue_w);
    if (tid < 8) *(u32*)(smem + K1_OFF_CNT + tid * 4) = 0;
    lbar();
    __builtin_amdgcn_s_setprio(2);
    asm volatile("s_waitcnt vmcnt(%1)" : "+v"(epoch) : "n"(2 * K1_EARLY) : "memory");
    STAMP(0, 1);
    center_x();
    soft_arrive(smem + K1_OFF_CNT + 4, lane);
    STAMP(0, 2);
    static_for<K1_EARLY, K1_RING>(issue_w);
    soft_wait(smem + K1_OFF_CNT + 4, 8);
    __builtin_amdgcn_s_setprio(3);
    STAMP(0, 3);

    f32x16 a2[2], a1[2];
#pragma unroll
    for (int i = 0; i < 16; ++i) { a2[0][i] = 0.f; a2[1][i] = 0.f; a1[0][i] = 0.f; a1[1][i] = 0.f; }
    {
        const int h = lane >> 5, blk = (lane >> 4) & 1, q = (lane & 15) >> 2, pp = lane & 3;
        const char* aptr = smem + K1_OFF_RING + ft * (K1_RING * 2048) + (8 * h + q) * 64 + 32 * blk + 8 * pp;
        const char* bptr = smem + (lane & 31) * K1_XSTRIDE + h * 16;
        u32x4 f2[3], f1[3], b0[3], b1[3];
        auto rd_a = [&](auto I) LAM {
            constexpr int ks = I.value, cur = ks % 3, so = (ks % K1_RING) * 2048;
            const u32x2 l2 = tr_read(aptr + so), h2 = tr_read(aptr + so + 256);
            f2[cur] = (u32x4){l2[0], l2[1], h2[0], h2[1]};
            b0[cur] = lds16(bptr + ks * 32);
        };
        auto rd_b = [&](auto I) LAM {
            constexpr int ks = I.value, cur = ks % 3, so = (ks % K1_RING) * 2048;
            b1[cur] = lds16(bptr + 32 * K1_XSTRIDE + ks * 32);
            const u32x2 l1 = tr_read(aptr + so + 1024), h1 = tr_read(aptr + so + 1280);
            f1[cur] = (u32x4){l1[0], l1[1], h1[0], h1[1]};
        };
        wait_vm<2 * (K1_RING - 1)>();
        rd_a(IC<0>{}); rd_b(IC<0>{});
        wait_vm<2 * (K1_RING - 2)>();
        rd_a(IC<1>{}); rd_b(IC<1>{});
        static_for<0, 28>([&](auto I) LAM {
            constexpr int ks = I.value, cur = ks % 3;
            sched_fence();
            a2[0] = mfma32_bf(f2[cur], b0[cur], a2[0]);
            sched_fence();
            if constexpr (ks + K1_RING < 28) issue_w1(IC<ks + K1_RING>{}, 0);
            sched_fence();
            a2[1] = mfma32_bf(f2[cur], b1[cur], a2[1]);
            sched_fence();
            if constexpr (ks + 2 < 28) {
                constexpr int issued = (ks + K1_RING - 1 < 27) ? ks + K1_RING - 1 : 27;
                constexpr int extra = (ks + K1_RING < 28) ? 1 : 0;
                wait_vm<2 * (issued - (ks + 2)) + extra>();
                rd_a(IC<ks + 2>{});
            }
            sched_fence();
            a1[0] = mfma32_bf(f1[cur], b0[cur], a1[0]);
            sched_fence();
            if constexpr (ks + K1_RING < 28) issue_w1(IC<ks + K1_RING>{}, 1);
            sched_fence();
            a1[1] = mfma32_bf(f1[cur], b1[cur], a1[1]);
            sched_fence();
            if constexpr (ks + 2 < 28) rd_b(IC<ks + 2>{});
        });
    }
    STAMP(0, 4);

    asm volatile("s_nop 7\n\ts_nop 7" : "+v"(a2[0]), "+v"(a2[1]), "+v"(a1[0]), "+v"(a1[1]));
    const int h = lane >> 5;
    float ri[2];
#pragma unroll
    for (int st = 0; st < 2; ++st) ri[st] = *(const float*)(smem + K1_OFF_ST + (32 * st + (lane & 31)) * 4);
    auto fold = [&](f32x16 (&acc)[2], int) LAM {
#pragma unroll
        for (int st = 0; st < 2; ++st)
#pragma unroll
            for (int i = 0; i < 16; ++i) acc[st][i] = smul(acc[st][i], ri[st]);
    };
    const int ftile = 4 * j + ft;
    fold(a2, 0);
    u32 zp[2][8];
    {
        float mw[2], m2[2];
#pragma unroll
        for (int st = 0; st < 2; ++st) {
            float s0 = 0.f, q0 = 0.f;
#pragma unroll
            for (int i = 0; i < 8; i += 2) {
                const u32 c = pkbf(a2[st][2 * i], a2[st][2 * i + 1]), c2 = pkbf(a2[st][2 * i + 2], a2[st][2 * i + 3]);
                float g0 = bflo(c), g1 = bfhi(c), g2 = bflo(c2), g3 = bfhi(c2);
                gelu4(g0, g1, g2, g3);
                zp[st][i] = pkbf(g0, g1); zp[st][i + 1] = pkbf(g2, g3);
                s0 = dot2bf(zp[st][i], 0x3F803F80u, s0); q0 = dot2bf(zp[st][i], zp[st][i], q0);
                s0 = dot2bf(zp[st][i + 1], 0x3F803F80u, s0); q0 = dot2bf(zp[st][i + 1], zp[st][i + 1], q0);
            }
            const float sum = addx32(s0), sq = addx32(q0);
            mw[st] = sum * (1.0f / 32.0f);
            m2[st] = __builtin_fmaxf(sq - sum * mw[st], 0.f);
        }
        f32x2 pv = {h ? mw[1] : mw[0], h ? m2[1] : m2[0]};
        *(f32x2*)(smem + K1_OFF_RING + ft * (K1_RING * 2048) + K1_PS_IN_RING + lane * 8) = pv;
    }
    STAMP(0, 5);
    const u32 tagA = epoch + TAG_C, tagB = tagA ^ TAG_X;
    char* gr = ws + OFF_GR + (size_t)p * 7 * 1024;
    const __amdgpu_buffer_rsrc_t grs = __builtin_amdgcn_make_buffer_rsrc(gr, 0, 7 * 1024, 0x00020000);
    {
        wait_lgkm0();
        u32 old = 0;
        if (lane == 0) old = __hip_atomic_fetch_add((LAS u32*)(smem + K1_OFF_CNT), 1u, __ATOMIC_RELAXED, __HIP_MEMORY_SCOPE_WORKGROUP);
        old = __builtin_amdgcn_readfirstlane(old);
        if (old == 3) {
            f32x2 a[4];
#pragma unroll
            for (int w = 0; w < 4; ++w) a[w] = *(const f32x2*)(smem + K1_OFF_RING + w * (K1_RING * 2048) + K1_PS_IN_RING + lane * 8);
            const float mean = (a[0][0] + a[1][0] + a[2][0] + a[3][0]) * 0.25f;
            float m2 = a[0][1] + a[1][1] + a[2][1] + a[3][1];
#pragma unroll
            for (int w = 0; w < 4; ++w) { const float dlt = a[w][0] - mean; m2 = __builtin_fmaf(32.0f * dlt, dlt, m2); }
            u32x4 g = {__builtin_bit_cast(u32, mean), tagA, __builtin_bit_cast(u32, m2), tagB};
            __builtin_amdgcn_raw_buffer_store_b128(g, grs, j * 1024 + lane * 16, 0, 16);
        }
    }
    __builtin_amdgcn_s_setprio(1);
    STAMP(0, 6);
    u32x4 g[7], g2[7], z1p[2][2], xnp[2];
    fold(a1, 1);
    {
#pragma unroll
        for (int st = 0; st < 2; ++st)
#pragma unroll
            for (int hf = 0; hf < 2; ++hf) {
                if (st == 1 && hf == 1) {
                    sched_fence();
#pragma unroll
                    for (int w = 0; w < 7; ++w) g[w] = __builtin_amdgcn_raw_buffer_load_b128(grs, w * 1024 + lane * 16, 0, 16);
                    sched_fence();
                }
                u32x4 v;
#pragma unroll
                for (int k = 0; k < 4; k += 2) {
                    const u32 c = pkbf(a1[st][8 * hf + 2 * k], a1[st][8 * hf + 2 * k + 1]), c2 = pkbf(a1[st][8 * hf + 2 * k + 2], a1[st][8 * hf + 2 * k + 3]);
                    float g0 = bflo(c), g1 = bfhi(c), g2 = bflo(c2), g3 = bfhi(c2);
                    gelu4(g0, g1, g2, g3);
                    v[k] = pkbf(g0, g1); v[k + 1] = pkbf(g2, g3);
                }
                z1p[st][hf] = v;
            }
    }
    sched_fence();
#pragma unroll
    for (int w = 0; w < 7; ++w) g2[w] = __builtin_amdgcn_raw_buffer_load_b128(grs, w * 1024 + lane * 16, 0, 16);
    sched_fence();
    {
        const int g = lane >> 4, q = (lane & 15) >> 2, pp = lane & 3;
#pragma unroll
        for (int sh = 0; sh < 2; ++sh) {
            const int dbase = 64 * j + 16 * ft;
            const char* a = smem + (32 * sh + 8 * g + q) * K1_XSTRIDE + (dbase + 4 * pp) * 2;
            const u32x2 lo = tr_read(a), hi = tr_read(a + 4 * K1_XSTRIDE);
            const u32 raw[4] = {lo[0], lo[1], hi[0], hi[1]};
            const f32x4 r0 = *(const f32x4*)(smem + K1_OFF_ST + (32 * sh + 8 * g) * 4), r1 = *(const f32x4*)(smem + K1_OFF_ST + (32 * sh + 8 * g + 4) * 4);
#pragma unroll
            for (int k = 0; k < 4; ++k) {
                const float ra = k < 2 ? r0[2 * k] : r1[2 * k - 4], rb = k < 2 ? r0[2 * k + 1] : r1[2 * k - 3];
                xnp[sh][k] = pkbf(bflo(raw[k]) * ra, bfhi(raw[k]) * rb);
            }
        }
    }
    STAMP(0, 7);
    __builtin_amdgcn_s_setprio(3);
    float mb, rinv;
    {
        for (int spin = 0; spin < (1 << 22); ++spin) {
            u32 bad = 0;
#pragma unroll
            for (int w = 0; w < 7; ++w) bad |= (g[w][1] ^ tagA) | (g[w][3] ^ tagB);
            if (__all(bad == 0)) break;
            asm volatile("" ::: "memory");
            if (spin == 0) {
#pragma unroll
                for (int w = 0; w < 7; ++w) g[w] = g2[w];
            } else {
#pragma unroll
                for (int w = 0; w < 7; ++w) g[w] = __builtin_amdgcn_raw_buffer_load_b128(grs, w * 1024 + lane * 16, 0, 16);
            }
        }
        float mean = 0.f, m2 = 0.f;
#pragma unroll
        for (int w = 0; w < 7; ++w) { mean += __builtin_bit_cast(float, (u32)g[w][0]); m2 += __builtin_bit_cast(float, (u32)g[w][2]); }
        mean *= (1.0f / 7.0f);
#pragma unroll
        for (int w = 0; w < 7; ++w) { const float dlt = __builtin_bit_cast(float, (u32)g[w][0]) - mean; m2 = __builtin_fmaf(128.0f * dlt, dlt, m2); }
        mb = rbf(mean);
        const float vb = rbf(m2 * (1.0f / FH));
        rinv = __builtin_amdgcn_rcpf(rbf(__builtin_amdgcn_sqrtf(rbf(vb + 1e-5f))));
        if (j == 0 && ft == 0 && lane == 0)
            __hip_atomic_store((u32*)(ws + OFF_EP + p * 128), tagA, __ATOMIC_RELAXED, __HIP_MEMORY_SCOPE_AGENT);
    }
    STAMP(0, 8);
    {
        char* tb = smem + K1_OFF_RING + ft * (K1_RING * 2048);
        u32 mbb = __builtin_bit_cast(u32, mb), rib = __builtin_bit_cast(u32, rinv);
        auto sm = __builtin_amdgcn_permlane32_swap(mbb, mbb, false, false);
        auto sr = __builtin_amdgcn_permlane32_swap(rib, rib, false, false);
#pragma unroll
        for (int st = 0; st < 2; ++st) {
            const float m = __builtin_bit_cast(float, (u32)sm[st]), r = __builtin_bit_cast(float, (u32)sr[st]);
#pragma unroll
            for (int g = 0; g < 4; ++g) {
                u32x2 v;
#pragma unroll
                for (int k = 0; k < 2; ++k) {
                    const u32 zz = zp[st][2 * g + k];
                    const u32 c = pkbf(bflo(zz) - m, bfhi(zz) - m);
                    v[k] = pkbf(bflo(c) * r, bfhi(c) * r);
                }
                *(u32x2*)(tb + (32 * st + (lane & 31)) * 72 + (8 * g + 4 * h) * 2) = v;
            }
        }
        const int blk = (lane >> 4) & 1, q = (lane & 15) >> 2, pp = lane & 3;
        char* z2t = ws + OFF_Z2T;
#pragma unroll
        for (int ks = 0; ks < 4; ++ks) {
            const char* a = tb + (16 * ks + 8 * h + q) * 72 + 32 * blk + 8 * pp;
            const u32x2 lo = tr_read(a), hi = tr_read(a + 4 * 72);
            gst16m<ST_LATE>(z2t + ((size_t)(ftile * 128 + 4 * p + ks) * 64 + lane) * 16, (u32x4){lo[0], lo[1], hi[0], hi[1]});
        }
        char* z1 = ws + OFF_Z1;
#pragma unroll
        for (int st = 0; st < 2; ++st)
#pragma unroll
            for (int hf = 0; hf < 2; ++hf) gst16m<ST_LATE>(z1 + ((size_t)((ftile * 64 + 2 * p + st) * 2 + hf) * 64 + lane) * 16, z1p[st][hf]);
        u16* xnt = (u16*)(ws + OFF_XNT);
#pragma unroll
        for (int sh = 0; sh < 2; ++sh)
            gst16m<ST_LATE>(xnt + (size_t)(64 * j + 16 * ft + (lane & 15)) * SL + 64 * p + 32 * sh + 8 * (lane >> 4), xnp[sh]);
    }
    STAMP(0, 14);
}

constexpr int CV_CS = 322 * 16;
constexpr int CV_OFF_KR = 8 * CV_CS;
#define G2D 5
#define K2W 3
constexpr int K2_OFF_SINK = (G2D + 1) * 8192;
constexpr int K2_LDS = K2_OFF_SINK + 1024;
constexpr int CV_OFF_PB = 0;
constexpr int G2_SLOTS = G2D + 1;
constexpr int G2_ZD = G2D;
constexpr int g2_ops(int i) { return (i + G2_SLOTS - 1 < 32 ? 2 : 0) + (i + G2_ZD < 32 ? 4 : 0) + (i == 27 ? 6 : 0); }
constexpr int g2_younger(int c) {
    int n = 0;
    const int wi = (c + 1) - (G2_SLOTS - 1);
    const int zi = c - G2_ZD;
    const bool need_w = c + 1 < 32;
    if (need_w && wi >= 0 && wi > zi) {
        n = g2_ops(wi) - 2;
        for (int i = wi + 1; i < c; ++i) n += g2_ops(i);
        return n;
    }
    if (zi >= 0 && (!need_w || zi >= wi)) {
        n = (zi == 27 ? 6 : 0);
        for (int i = zi + 1; i < c; ++i) n += g2_ops(i);
        return n;
    }
    const int lastp = need_w ? 6 * (c + 1) + 1 : 6 * c + 5;
    n = 6 * G2_ZD - 1 - lastp;
    for (int i = 0; i < c; ++i) n += g2_ops(i);
    return n;
}
static_assert(G2_SLOTS - 1 == G2_ZD, "prologue assumes equal depth");

#define KM(c) (c)
DI void silu4(float& x0, float& x1, float& x2, float& x3) {
    const float c = -1.4426950408889634f;
    float t0, t1, t2, t3;
    asm("v_mul_f32 %4, %8, %0\n\tv_mul_f32 %5, %8, %1\n\tv_mul_f32 %6, %8, %2\n\tv_mul_f32 %7, %8, %3\n\t"
        "v_exp_f32 %4, %4\n\tv_exp_f32 %5, %5\n\tv_exp_f32 %6, %6\n\tv_exp_f32 %7, %7\n\t"
        "v_add_f32 %4, 1.0, %4\n\tv_add_f32 %5, 1.0, %5\n\tv_add_f32 %6, 1.0, %6\n\tv_add_f32 %7, 1.0, %7\n\t"
        "v_rcp_f32 %4, %4\n\tv_rcp_f32 %5, %5\n\tv_rcp_f32 %6, %6\n\tv_rcp_f32 %7, %7\n\t"
        "v_mul_f32 %0, %0, %4\n\tv_mul_f32 %1, %1, %5\n\tv_mul_f32 %2, %2, %6\n\tv_mul_f32 %3, %3, %7"
        : "+v"(x0), "+v"(x1), "+v"(x2), "+v"(x3), "=&v"(t0), "=&v"(t1), "=&v"(t2), "=&v"(t3)
        : "v"(c));
}

constexpr int G3_AD = 6;
constexpr int G3_BD = 9;
constexpr int g3_younger(int k) {
    int pos = 0, need = 0;
    for (int q = 2; q < 2 + G3_BD; ++q) { pos += 2; if (q == k) need = pos; }
    for (int i = 2; i < k; ++i) {
        if (i + G3_AD < 14) { pos += 2; if (i + G3_AD == k + 1) need = pos > need ? pos : need; }
        if (i + G3_BD < 14) { pos += 2; if (i + G3_BD == k) need = pos > need ? pos : need; }
        if (i == 4) pos += 1;
        if (i == 7) pos += 20;
    }
    return pos - need;
}
DI void gemm2_role(int b, char* smem, const u16* w_sp, const u16* b_sp, const u16* x, u16* out, char* ws) {
    const int tid = threadIdx.x, lane = tid & 63;
    const int wid = __builtin_amdgcn_readfirstlane(tid >> 6);
    int tp, j;
    tile_map(b, tp, j);
    const int ftile = 4 * j + wid;
    const int qa = wid >> 1, qc = wid & 1;
    const u32 lds0 = (u32)(size_t)smem;
    STAMP_DECL;
    STAMP(1, 0);
#define G2_PRIO 3
    __builtin_amdgcn_s_setprio(G2_PRIO);
    u32 wvoff[2];
#pragma unroll
    for (int i = 0; i < 2; ++i) {
        const int r = 16 * wid + 8 * i + (lane >> 3), cc = (lane & 7) ^ ((r >> 1) & 7);
        wvoff[i] = (u32)(((64 * tp + r) * SL + cc * 8) * 2);
    }
    const char* z2base = ws + OFF_Z2T + (size_t)ftile * 128 * 1024;
    const u32 zvoff = lane * 16;
    u32x4 zf[G2_ZD][4];
    u32x4 z1v[2][2];
    u32 bsv[2];
    auto issue_w1 = [&](auto C, int i) LAM {
        constexpr int c = C.value, slot = c % G2_SLOTS;
        glds16((const char*)w_sp + KM(c) * 128, wvoff[i], lds0 + slot * 8192 + wid * 2048 + i * 1024);
    };
    static_for<0, G2_ZD>([&](auto C) LAM {
        constexpr int c = C.value;
        issue_w1(C, 0); issue_w1(C, 1);
        gload4x(zf[c][0], zf[c][1], zf[c][2], zf[c][3], zvoff, z2base + KM(c) * 4096);
    });
    f32x16 acc[2];
#pragma unroll
    for (int i = 0; i < 16; ++i) { acc[0][i] = 0.f; acc[1][i] = 0.f; }
    const int h = lane >> 5, tl = lane & 31;
    u32 roff[2][4];
#pragma unroll
    for (int tt = 0; tt < 2; ++tt)
#pragma unroll
        for (int kk = 0; kk < 4; ++kk) { const int r = 32 * (tt ^ qc) + tl; roff[tt][kk] = r * 128 + (((2 * kk + h) ^ ((r >> 1) & 7)) * 16); }
    u32x4 wa[2][2], wb[2][2];
    auto read_w = [&](u32x4 (&dst)[2][2], int slot, int kk0) LAM {
#pragma unroll
        for (int kk = 0; kk < 2; ++kk)
#pragma unroll
            for (int tt = 0; tt < 2; ++tt) dst[kk][tt] = lds16(smem + slot * 8192 + roff[tt][kk0 + kk]);
    };
    wait_vm<6 * G2_ZD - 2>();
    bar();
    read_w(wa, 0, 0);
    STAMP(1, 1);
    static_for<0, 32>([&](auto C) LAM {
        constexpr int c = C.value, set = c % G2_ZD;
        wait_vm_tie4<g2_younger(c)>(zf[set][0], zf[set][1], zf[set][2], zf[set][3]);
        if constexpr (c == 8) STAMP(1, 2);
        if constexpr (c == 16) STAMP(1, 3);
        if constexpr (c == 24) STAMP(1, 4);
        lbar();
        constexpr bool more_w = c + G2_SLOTS - 1 < 32;
        constexpr bool more_z = c + G2_ZD < 32;
        const char* zn = z2base + KM(c + G2_ZD) * 4096;
        if constexpr (more_w) { issue_w1(IC<c + G2_SLOTS - 1>{}, 0); issue_w1(IC<c + G2_SLOTS - 1>{}, 1); }
        read_w(wb, c % G2_SLOTS, 2);
        sched_fence();
#define G2M(kk, tt, W) acc[tt] = mfma32_bf(zf[set][kk], W, acc[tt])
        G2M(0, 0, wa[0][0]);
        G2M(0, 1, wa[0][1]);
        sched_fence();
        if constexpr (more_z) gload1(zf[set][0], zvoff, zn);
        G2M(1, 0, wa[1][0]);
        G2M(1, 1, wa[1][1]);
        sched_fence();
        if constexpr (more_z) gload1(zf[set][1], zvoff, zn + 1024);
        if constexpr (c + 1 < 32) read_w(wa, (c + 1) % G2_SLOTS, 0);
        sched_fence();
        G2M(2, 0, wb[0][0]);
        G2M(2, 1, wb[0][1]);
        G2M(3, 0, wb[1][0]);
        G2M(3, 1, wb[1][1]);
        sched_fence();
        if constexpr (more_z) { gload1(zf[set][2], zvoff, zn + 2048); gload1(zf[set][3], zvoff, zn + 3072); }
        if constexpr (c == 27) {
            const char* z1 = ws + OFF_Z1 + (size_t)(ftile * 64 + 2 * tp) * 2048;
            gload1(z1v[0][0], zvoff, z1 + qc * 2048); gload1(z1v[0][1], zvoff, z1 + qc * 2048 + 1024);
            gload1(z1v[1][0], zvoff, z1 + (qc ^ 1) * 2048); gload1(z1v[1][1], zvoff, z1 + (qc ^ 1) * 2048 + 1024);
            gload_u16(bsv[0], (u32)((64 * tp + 32 * qc + tl) * 2), (const char*)b_sp);
            gload_u16(bsv[1], (u32)((64 * tp + 32 * (qc ^ 1) + tl) * 2), (const char*)b_sp);
        }
    });
    STAMP(1, 5);
    asm volatile("s_waitcnt vmcnt(0)\n\ts_nop 7\n\ts_nop 7" : "+v"(acc[0]), "+v"(acc[1]), "+v"(z1v[0][0]), "+v"(z1v[0][1]), "+v"(z1v[1][0]), "+v"(z1v[1][1]), "+v"(bsv[0]), "+v"(bsv[1]));
    char* ht = ws + OFF_HT;
    u32x4 hv[2][2];
#pragma unroll
    for (int tt = 0; tt < 2; ++tt) {
        const float bs = bf1((u16)bsv[tt]);
#pragma unroll
        for (int s = 0; s < 2; ++s) {
            u32x4 v;
#pragma unroll
            for (int k = 0; k < 4; ++k) {
                const u32 g = pkbf(acc[tt][8 * s + 2 * k], acc[tt][8 * s + 2 * k + 1]);
                const u32 g2 = pkbf(bflo(g) + bs, bfhi(g) + bs);
                const u32 z = z1v[tt][s][k];
                v[k] = pkbf(bflo(z) * bflo(g2), bfhi(z) * bfhi(g2));
            }
            hv[tt][s] = v;
            gst16_dev(ht + ((size_t)(((2 * tp + (tt ^ qc)) * 28 + ftile) * 2 + s) * 64 + lane) * 16, v);
        }
    }
    auto ft_off = [&](int k) LAM { int q = j + (k >> 1); q = q >= 7 ? q - 7 : q; return ((4 * q + wid) * 2 + (k & 1)) * 1024; };
    const char* wo0 = ws + OFF_WO + (size_t)((2 * j + qa) * 56) * 1024;
    const char* wo1 = ws + OFF_WO + (size_t)((2 * j + (qa ^ 1)) * 56) * 1024;
    const char* ht0 = ws + OFF_HT + (size_t)((2 * tp + qc) * 56) * 1024;
    const char* ht1 = ws + OFF_HT + (size_t)((2 * tp + (qc ^ 1)) * 56) * 1024;
    bar();
    const u32 aring = lds0 + wid * (G3_AD * 2048);
    auto issue_a = [&](auto K) LAM {
        constexpr int k = K.value, slot = k % G3_AD;
        const int o = ft_off(k);
        glds16(wo0 + o, zvoff, aring + slot * 2048);
        glds16(wo1 + o, zvoff, aring + slot * 2048 + 1024);
    };
    static_for<0, G3_AD>(issue_a);
    wait_vm<2 * G3_AD>();
    u32* fl = (u32*)(ws + OFF_FL);
    if (lane == 0) __hip_atomic_store(fl + tp * 32 + ftile, 1u, __ATOMIC_RELAXED, __HIP_MEMORY_SCOPE_AGENT);
    STAMP(1, 6);
    f32x16 a3[2][2];
#pragma unroll
    for (int a = 0; a < 2; ++a)
#pragma unroll
        for (int c = 0; c < 2; ++c)
#pragma unroll
            for (int i = 0; i < 16; ++i) a3[a][c][i] = 0.f;
    const char* ard = smem + wid * (G3_AD * 2048) + lane * 16;
    u32x4 ga[2][2];
    {
        wait_vm<2 * G3_AD - 4>();
        u32x4 g0[2][2];
#pragma unroll
        for (int s = 0; s < 2; ++s) { g0[s][0] = lds16(ard + s * 2048); g0[s][1] = lds16(ard + s * 2048 + 1024); }
        sched_fence();
#pragma unroll
        for (int s = 0; s < 2; ++s) {
            a3[0][0] = mfma32_bf(g0[s][0], hv[0][s], a3[0][0]);
            a3[0][1] = mfma32_bf(g0[s][0], hv[1][s], a3[0][1]);
            a3[1][0] = mfma32_bf(g0[s][1], hv[0][s], a3[1][0]);
            a3[1][1] = mfma32_bf(g0[s][1], hv[1][s], a3[1][1]);
        }
        sched_fence();
        issue_a(IC<G3_AD>{}); issue_a(IC<G3_AD + 1>{});
    }
    for (int spin = 0; spin < (1 << 22); ++spin) {
        const u32 fh = __hip_atomic_load(fl + tp * 32 + 4 * (lane < 7 ? lane : j) + wid, __ATOMIC_RELAXED, __HIP_MEMORY_SCOPE_AGENT);
        if (__all(fh == 1u)) break;
        asm volatile("s_sleep 1" ::: "memory");
    }
    wait_vm<0>();
    STAMP(1, 7);
    u32x4 gb[G3_BD][2];
    static_for<2, 2 + G3_BD>([&](auto K) LAM {
        constexpr int k = K.value, r = (k - 2) % G3_BD;
        const int o = ft_off(k);
        gload1_dev(gb[r][0], zvoff, ht0 + o); gload1_dev(gb[r][1], zvoff, ht1 + o);
    });
    const int t3 = 64 * tp + 32 * qc + tl, d0 = 64 * j + 32 * qa + 4 * h;
    u32x2 xv[4];
    u32 sv[16];
    u32 sflag;
    ga[0][0] = lds16(ard + (2 % G3_AD) * 2048); ga[0][1] = lds16(ard + (2 % G3_AD) * 2048 + 1024);
    static_for<2, 14>([&](auto K) LAM {
        constexpr int k = K.value, r = (k - 2) % G3_BD, cur = k & 1;
        wait_vm_tie2<g3_younger(k)>(gb[r][0], gb[r][1]);
        if constexpr (k + 1 < 14) { ga[cur ^ 1][0] = lds16(ard + ((k + 1) % G3_AD) * 2048); ga[cur ^ 1][1] = lds16(ard + ((k + 1) % G3_AD) * 2048 + 1024); }
        sched_fence();
        a3[0][0] = mfma32_bf(ga[cur][0], gb[r][0], a3[0][0]);
        a3[0][1] = mfma32_bf(ga[cur][0], gb[r][1], a3[0][1]);
        a3[1][0] = mfma32_bf(ga[cur][1], gb[r][0], a3[1][0]);
        a3[1][1] = mfma32_bf(ga[cur][1], gb[r][1], a3[1][1]);
        sched_fence();
        if constexpr (k + G3_AD < 14) issue_a(IC<k + G3_AD>{});
        if constexpr (k + G3_BD < 14) {
            const int o = ft_off(k + G3_BD);
            gload1_dev(gb[r][0], zvoff, ht0 + o); gload1_dev(gb[r][1], zvoff, ht1 + o);
        }
        if constexpr (k == 4) gload_flag(sflag, (u32)((FL_S + 64 * j + 32 * qa + (lane & 31)) * 4), (const char*)fl);
        if constexpr (k == 7) {
            wait_vm_tie1<6>(sflag);
            if (!__all(sflag == 1u)) {
                for (int spin = 0; spin < (1 << 22); ++spin) {
                    const u32 fs = __hip_atomic_load(fl + FL_S + 64 * j + 32 * qa + (lane & 31), __ATOMIC_RELAXED, __HIP_MEMORY_SCOPE_AGENT);
                    if (__all(fs == 1u)) break;
                    asm volatile("s_sleep 1" ::: "memory");
                }
            }
            const u32 xo = (u32)((t3 * DM + d0) * 2), so = (u32)(((tp * DM + d0) * 64 + (t3 & 63)) * 2);
            gload_x4(xv, xo, (const char*)x);
            gload_s16(sv, so, ws + OFF_ST);
        }
    });
    STAMP(1, 8);
    asm volatile("s_waitcnt vmcnt(0)\n\ts_nop 7\n\ts_nop 7"
                 : "+v"(a3[0][0]), "+v"(a3[0][1]), "+v"(a3[1][0]), "+v"(a3[1][1]), "+v"(xv[0]), "+v"(xv[1]), "+v"(xv[2]), "+v"(xv[3]), "+v"(sv[0]),
                   "+v"(sv[1]), "+v"(sv[2]), "+v"(sv[3]), "+v"(sv[4]), "+v"(sv[5]), "+v"(sv[6]), "+v"(sv[7]), "+v"(sv[8]), "+v"(sv[9]),
                   "+v"(sv[10]), "+v"(sv[11]), "+v"(sv[12]), "+v"(sv[13]), "+v"(sv[14]), "+v"(sv[15])::"memory");
#pragma unroll
    for (int r = 1; r < 4; ++r) {
        const f32x16& v = a3[r >> 1][r & 1];
        char* dst = smem + wid * (G3_AD * 2048) + (r - 1) * 4096 + lane * 16;
#pragma unroll
        for (int g = 0; g < 4; ++g) *(f32x4*)(dst + g * 1024) = (f32x4){v[4 * g], v[4 * g + 1], v[4 * g + 2], v[4 * g + 3]};
    }
    lbar();
    STAMP(1, 9);
    float o[16];
    {
        f32x4 part[3][4];
#pragma unroll
        for (int r = 1; r < 4; ++r)
#pragma unroll
            for (int g = 0; g < 4; ++g) part[r - 1][g] = *(const f32x4*)(smem + (wid ^ r) * (G3_AD * 2048) + (r - 1) * 4096 + g * 1024 + lane * 16);
#pragma unroll
        for (int g = 0; g < 4; ++g)
#pragma unroll
            for (int i = 0; i < 4; ++i) o[4 * g + i] = (a3[0][0][4 * g + i] + part[1][g][i]) + (part[0][g][i] + part[2][g][i]);
    }
    u32x2 ov[4];
#pragma unroll
    for (int g = 0; g < 4; ++g) {
        const u32 c0 = pkbf(o[4 * g], o[4 * g + 1]), c1 = pkbf(o[4 * g + 2], o[4 * g + 3]);
        float g0 = bflo(c0), g1 = bfhi(c0), g2 = bflo(c1), g3 = bfhi(c1);
        silu4(g0, g1, g2, g3);
        const u32 q0 = pkbf(g0, g1), q1 = pkbf(g2, g3);
        const u32 p0 = pkbf(bflo(sv[4 * g]) * bflo(q0), bflo(sv[4 * g + 1]) * bfhi(q0));
        const u32 p1 = pkbf(bflo(sv[4 * g + 2]) * bflo(q1), bflo(sv[4 * g + 3]) * bfhi(q1));
        ov[g][0] = pkbf(bflo(xv[g][0]) + bflo(p0), bfhi(xv[g][0]) + bfhi(p0));
        ov[g][1] = pkbf(bflo(xv[g][1]) + bflo(p1), bfhi(xv[g][1]) + bfhi(p1));
    }
#pragma unroll
    for (int g = 0; g < 4; g += 2) {
        auto s0 = __builtin_amdgcn_permlane32_swap(ov[g][0], ov[g + 1][0], false, false);
        auto s1 = __builtin_amdgcn_permlane32_swap(ov[g][1], ov[g + 1][1], false, false);
        gst16m<ST_OUT>(out + (size_t)t3 * DM + 64 * j + 32 * qa + 8 * g + 8 * h, (u32x4){(u32)s0[0], (u32)s1[0], (u32)s0[1], (u32)s1[1]});
    }
    STAMP(1, 14);
}

template <int NS> DI void conv_seg(const char* smem, const char* kr, int lane, int tt, int e0, f32x4 (&acc)[4]) {
    constexpr int PD = 4;
    const int q = lane & 15, kg = lane >> 4, c = lane & 7, pl = (lane >> 3) & 1;
    const int m0 = -(32 * tt + 16) + e0;
    const char* ap = kr + (q - kg + 64) * 16 - 64 * m0 - 64 * (NS - 1);
    const char* bp = smem + c * CV_CS + (64 * (2 * tt + pl) + kg + 64) * 16 + 64 * m0;
    u32x4 F[NS + 12], B[NS];
#pragma unroll
    for (int n = 0; n < 12 + PD; ++n) F[n] = lds16(ap + 64 * (NS + 11 - n));
#pragma unroll
    for (int e = 0; e < PD; ++e) B[e] = lds16(bp + 64 * e);
#pragma unroll
    for (int a = 0; a < 4; ++a) acc[a] = (f32x4){0.f, 0.f, 0.f, 0.f};
    static_for<0, NS>([&](auto E) LAM {
        constexpr int e = E.value;
        if constexpr (e + PD < NS) { F[e + 12 + PD] = lds16(ap + 64 * (NS + 11 - (e + 12 + PD))); B[e + PD] = lds16(bp + 64 * (e + PD)); }
        sched_fence();
#pragma unroll
        for (int a = 0; a < 4; ++a) acc[a] = mfma16_h(F[e + 12 - 4 * a], B[e], acc[a]);
        sched_fence();
    });
}

DI void conv_ucopies(char* smem, int tid, u32x4 uprev, u32x4 ucur) {
    u32 v[8];
#pragma unroll
    for (int k = 0; k < 4; ++k) { v[k] = pkh(bflo(uprev[k]), bfhi(uprev[k])); v[4 + k] = pkh(bflo(ucur[k]), bfhi(ucur[k])); }
#pragma unroll
    for (int c = 0; c < 8; ++c) {
        u32x4 o;
#pragma unroll
        for (int k = 0; k < 4; ++k) {
            const int e = c + 1 + 2 * k;
            o[k] = (e & 1) ? __builtin_amdgcn_alignbyte(v[(e + 1) >> 1 < 8 ? (e + 1) >> 1 : 7], v[e >> 1], 2) : v[e >> 1];
        }
        *(u32x4*)(smem + c * CV_CS + (tid + 64) * 16) = o;
    }
}
DI void conv_run(const char* smem, const char* kr, int wid, int lane, f32x4 (&ca)[4], f32x4 (&cb)[4]) {
    if (wid == 0) conv_seg<24>(smem, kr, lane, 1, 0, ca);
    else if (wid == 1) conv_seg<24>(smem, kr, lane, 1, 24, ca);
    else if (wid == 2) { conv_seg<16>(smem, kr, lane, 1, 48, ca); conv_seg<8>(smem, kr, lane, 0, 0, cb); }
    else conv_seg<24>(smem, kr, lane, 0, 8, ca);
}
DI void conv_put(char* smem, int wid, int lane, const f32x4 (&ca)[4], const f32x4 (&cb)[4]) {
    const int kg = lane >> 4, c = lane & 7, pl = (lane >> 3) & 1;
    float* pb = (float*)(smem + CV_OFF_PB) + 512 * pl + c + 32 * kg;
    float* p0 = pb + 1024 * (wid == 3 ? 4 : wid);
#pragma unroll
    for (int a = 0; a < 4; ++a)
#pragma unroll
        for (int r = 0; r < 4; ++r) p0[128 * a + 8 * r] = ca[a][r];
    if (wid == 2) {
#pragma unroll
        for (int a = 0; a < 4; ++a)
#pragma unroll
            for (int r = 0; r < 4; ++r) pb[3 * 1024 + 128 * a + 8 * r] = cb[a][r];
    }
}
DI void conv_out(const char* smem, int tid, int d, float kinv, float dsk, u32x4 ucur, char* ws) {
    const char* yb = smem + CV_OFF_PB + ((8 * tid) & 1023) * 4;
    f32x4 y0, y1;
    if (tid < 128) {
        y0 = *(const f32x4*)(yb + 3 * 4096) + *(const f32x4*)(yb + 4 * 4096);
        y1 = *(const f32x4*)(yb + 3 * 4096 + 16) + *(const f32x4*)(yb + 4 * 4096 + 16);
    } else {
        y0 = (*(const f32x4*)(yb) + *(const f32x4*)(yb + 4096)) + *(const f32x4*)(yb + 2 * 4096);
        y1 = (*(const f32x4*)(yb + 16) + *(const f32x4*)(yb + 4096 + 16)) + *(const f32x4*)(yb + 2 * 4096 + 16);
    }
    u32x4 o;
#pragma unroll
    for (int k = 0; k < 4; ++k) {
        const float ya = k < 2 ? y0[2 * k] : y1[2 * k - 4], yb2 = k < 2 ? y0[2 * k + 1] : y1[2 * k - 3];
        const u32 y = pkbf(ya * kinv, yb2 * kinv);
        const u32 sk = pkbf(bflo(ucur[k]) * dsk, bfhi(ucur[k]) * dsk);
        o[k] = pkbf(bflo(y) + bflo(sk), bfhi(y) + bfhi(sk));
    }
    gst16_dev((u16*)(ws + OFF_ST) + ((size_t)(tid >> 3) * DM + d) * 64 + (tid & 7) * 8, o);
    wait_vm<0>();
    bar();
    if (tid == 0) __hip_atomic_store((u32*)(ws + OFF_FL) + FL_S + d, 1u, __ATOMIC_RELAXED, __HIP_MEMORY_SCOPE_AGENT);
}

template <bool GEN>
DI void conv_role(int d, char* smem, const u16* logA, const u16* Aim, const u16* Cre, const u16* Cim, const u16* logdt, const u16* Dsk,
                  char* ws) {
    const int tid = threadIdx.x, lane = tid & 63;
    const int wid = __builtin_amdgcn_readfirstlane(tid >> 6);
    STAMP_DECL;
    STAMP(1, 0);
    const u32 lds0 = (u32)(size_t)smem;
    if constexpr (GEN) {
        if (d < 8) {
            const char* cb;
            CODE_ADDR(cb, k2_kernel);
            code_touch(cb, wid, lane, lds0 + K2_OFF_SINK);
            if (wid == 0) code_touch(cb, 4, lane, lds0 + K2_OFF_SINK);
        }
    }
    KRaw pr;
    if constexpr (GEN) kgen_load(pr, d, tid, logA, Aim, Cre, Cim, logdt);
    else glds16(ws + OFF_KR + (size_t)d * 4096 + wid * 1024, lane * 16, lds0 + CV_OFF_KR + (64 + 64 * wid) * 16);
    const u16* xnt = (const u16*)(ws + OFF_XNT) + (size_t)d * SL;
    u32x4 ucur, uprev;
    u32 dskr, kir;
    {
        const u32 vo = tid * 16, vp = tid > 0 ? tid * 16 - 16 : 0;
        asm volatile("global_load_dwordx4 %0, %4, %6\n\tglobal_load_dwordx4 %1, %5, %6\n\tglobal_load_ushort %2, %7, %8\n\tglobal_load_dword %3, %7, %9"
                     : "=&v"(ucur), "=&v"(uprev), "=&v"(dskr), "=&v"(kir)
                     : "v"(vo), "v"(vp), "s"(xnt), "v"(0u), "s"(Dsk + d), "s"(ws + OFF_KI + d * 4)
                     : "memory");
    }
    if (tid < 64) *(u32x4*)(smem + CV_OFF_KR + tid * 16) = (u32x4){0u, 0u, 0u, 0u};
    float kinv = 0.f;
    if constexpr (GEN) {
        kgen_wait<4>(pr);
        kinv = kgen_wave_raw(smem + wid * CV_STG, lane, pr);
        lbar();
        kgen_reduce(smem, smem + 4 * CV_STG, tid);
        lbar();
        *(u32x4*)(smem + CV_OFF_KR + (tid + 64) * 16) = kgen_expand(smem + 4 * CV_STG, tid);
        lbar();
    }
#pragma unroll
    for (int i = 0; i < 2; ++i) {
        const int zc = tid + 256 * i;
        *(u32x4*)(smem + (zc >> 6) * CV_CS + (zc & 63) * 16) = (u32x4){0u, 0u, 0u, 0u};
    }
    asm volatile("s_waitcnt vmcnt(0)" : "+v"(ucur), "+v"(uprev), "+v"(dskr), "+v"(kir)::"memory");
    if constexpr (!GEN) kinv = __builtin_bit_cast(float, kir);
    if (tid == 0) uprev = (u32x4){0u, 0u, 0u, 0u};
    const float dsk = bf1((u16)dskr);
    conv_ucopies(smem, tid, uprev, ucur);
    STAMP(1, 3);
    lbar();
    STAMP(1, 4);
    f32x4 ca[4], cb[4];
    conv_run(smem, smem + CV_OFF_KR, wid, lane, ca, cb);
    STAMP(1, 5);
    lbar();
    conv_put(smem, wid, lane, ca, cb);
    lbar();
    STAMP(1, 6);
    conv_out(smem, tid, d, kinv, dsk, ucur, ws);
    if (wid == 0) {
        const int bb = d + 224, side = bb & 1, line = (bb >> 3) * 64 - 28 * 64 + lane;
        const int jj = line / 896, jg = side ? (jj < 3 ? 4 + jj : 3) : jj;
        glds4(ws + OFF_WO, (u32)(jg * 114688 + (line - jj * 896) * 128), (u32)(size_t)smem + K2_OFF_SINK);
    }
    STAMP(1, 14);
}

extern "C" __global__ __launch_bounds__(256, K2W) void k2_kernel(const u16* w_sp, const u16* b_sp, const u16* logA, const u16* Aim, const u16* Cre,
                                                    const u16* Cim, const u16* logdt, const u16* Dsk, const u16* x, u16* out, char* ws) {
    extern __shared__ __attribute__((aligned(16))) char smem[];
    const int b = blockIdx.x;
    if (b < 224) gemm2_role(b, smem, w_sp, b_sp, x, out, ws);
    if (b >= 224) {
        const int d = b - 224;
        if (kgen_in_k2(d)) conv_role<true>(d, smem, logA, Aim, Cre, Cim, logdt, Dsk, ws);
        else conv_role<false>(d, smem, logA, Aim, Cre, Cim, logdt, Dsk, ws);
    }
}

extern "C" void kernel_launch(void* const* d_in, const int* in_sizes, int n_in, void* d_out, int out_size, void* d_ws, size_t ws_size,
                              hipStream_t stream) {
    const u16* x = (const u16*)d_in[0];
    const u16* logA = (const u16*)d_in[1];
    const u16* Aim = (const u16*)d_in[2];
    const u16* Cre = (const u16*)d_in[3];
    const u16* Cim = (const u16*)d_in[4];
    const u16* logdt = (const u16*)d_in[5];
    const u16* Dsk = (const u16*)d_in[6];
    const u16* w_in = (const u16*)d_in[8];
    const u16* w_sp = (const u16*)d_in[9];
    const u16* b_sp = (const u16*)d_in[10];
    const u16* w_out = (const u16*)d_in[11];
    char* ws = (char*)d_ws;
    hipFuncSetAttribute((const void*)k1_kernel, hipFuncAttributeMaxDynamicSharedMemorySize, K1_LDS);
    hipFuncSetAttribute((const void*)k2_kernel, hipFuncAttributeMaxDynamicSharedMemorySize, K2_LDS);
    k1_kernel<<<256, 512, K1_LDS, stream>>>(x, w_in, w_out, w_sp, b_sp, logA, Aim, Cre, Cim, logdt, ws);
    k2_kernel<<<224 + 448, 256, K2_LDS, stream>>>(w_sp, b_sp, logA, Aim, Cre, Cim, logdt, Dsk, x, (u16*)d_out, ws);
}
```
